```python
import torch

def _softmax(x, axis=-1):
    return torch.softmax(x, dim=axis)
def _gelu(x):
    return torch.nn.functional.gelu(x, approximate="tanh")
def _silu(x):
    return torch.nn.functional.silu(x)
def _sigmoid(x):
    return torch.sigmoid(x)
def _layernorm(x, axis=-1, eps=1e-5):
    m = x.mean(dim=axis, keepdim=True)
    v = x.var(dim=axis, keepdim=True, unbiased=False)
    return (x - m) / torch.sqrt(v + eps)
def _scatter_add(tgt, idx, src, axis=0):
    return tgt.clone().index_add_(axis, idx, src)
def _segment_sum(src, seg, n):
    out = torch.zeros((n,) + src.shape[1:], dtype=src.dtype)
    return out.index_add_(0, seg, src)
def _pad(x, pw):
    flat = tuple(v for pair in reversed(pw) for v in pair)
    return torch.nn.functional.pad(x, flat)

kernel_name = 'gated_mix__softmax_full_mha__dense_activation_mish__prompt'

def setup_inputs():

    _w = (lambda a: (lambda t: t.to(torch.bfloat16) if t.is_floating_point() else t)(torch.as_tensor(a)))
    def _A_setup():
        B, S, D = 1, 2048, 512
        return {
            "x": _w((lambda *s: torch.randn(*s, dtype=torch.bfloat16) * 0.05)(B, S, D)),
            "wqkv": _w((lambda *s: torch.randn(*s, dtype=torch.bfloat16) * 0.8)(D, 3 * D)),
            "wo": _w((lambda *s: torch.randn(*s, dtype=torch.bfloat16) * 0.05)(D, D)),
        }
    _A_in = _A_setup()

    def _B_setup():
        B, S, D, F = 1, 2048, 512, 1792
        return {
            "x": _w((lambda *s: torch.randn(*s, dtype=torch.bfloat16) * 0.05)(B, S, D)),
            "w1": _w((lambda *s: torch.randn(*s, dtype=torch.bfloat16) * 12.8)(D, F) * (D ** -0.5)),
            "b1": _w((lambda *s: torch.randn(*s, dtype=torch.bfloat16) * 0.2)(F)),
            "w2": _w((lambda *s: torch.randn(*s, dtype=torch.bfloat16) * 0.1)(F, D) * (F ** -0.5)),
        }
    _B_in = _B_setup()

    return {
        "A_x": _A_in["x"],
        "A_wqkv": _A_in["wqkv"],
        "A_wo": _A_in["wo"],
        "B_x": _B_in["x"],
        "B_w1": _B_in["w1"],
        "B_b1": _B_in["b1"],
        "B_w2": _B_in["w2"],
        "gate_w": _w((lambda *s: torch.randn(*s, dtype=torch.bfloat16) * 0.05)(512) * 16.0),
    }

def reference(A_x, A_wqkv, A_wo, B_x, B_w1, B_b1, B_w2, gate_w):
    def _A_ref(x, wqkv, wo):
        B, S, D = x.shape
        H = 32; Dh = D // H
        qkv = (lambda x, *d: x.permute(*d))((x @ wqkv).reshape(B, S, 3, H, Dh), 2, 0, 3, 1, 4)
        q, k, v = qkv[0], qkv[1], qkv[2]
        scale = Dh ** -0.5
        scores = torch.einsum("bhqd,bhkd->bhqk", q, k) * scale
        p = _softmax(scores, axis=-1)
        o = torch.einsum("bhqk,bhkd->bhqd", p, v)
        return (lambda x, *d: x.permute(*d))(o, 0, 2, 1, 3).reshape(B, S, D) @ wo

    def _B_ref(x, w1, b1, w2):
        h = torch.einsum("bsd,df->bsf", x, w1) + b1
        sp = torch.log1p(torch.exp(-torch.abs(h))) + torch.where(h > 0, h, torch.zeros_like(h))
        act = h * torch.tanh(sp)
        return torch.einsum("bsf,fd->bsd", act, w2)

    x = A_x
    g = _sigmoid((x * gate_w).sum(-1, keepdims=True))
    a = _A_ref(x, A_wqkv, A_wo)
    b = _B_ref(x, B_w1, B_b1, B_w2)
    return g * a + (1 - g) * b
```

```cpp
#include <hip/hip_runtime.h>
#include <hip/hip_bf16.h>
#include <stdint.h>

typedef __bf16 bf16x8 __attribute__((ext_vector_type(8)));
typedef __bf16 bf16x4 __attribute__((ext_vector_type(4)));
typedef __bf16 bf16x2 __attribute__((ext_vector_type(2)));
typedef short s16x4 __attribute__((ext_vector_type(4)));
typedef float f32x2 __attribute__((ext_vector_type(2)));
typedef float f32x4 __attribute__((ext_vector_type(4)));
typedef float f32x16 __attribute__((ext_vector_type(16)));
typedef unsigned u32x4 __attribute__((ext_vector_type(4)));
typedef unsigned u32x2 __attribute__((ext_vector_type(2)));
#define LAS __attribute__((address_space(3)))
#define DI __device__ __forceinline__

constexpr int S = 2048, D = 512, H = 32, F = 1792;
constexpr float LOG2E = 1.4426950408889634f;

constexpr size_t WS_Q = 0;
constexpr size_t WS_K = WS_Q + (2u << 20);
constexpr size_t WS_V = WS_K + (2u << 20);
constexpr size_t WS_O = WS_V + (2u << 20);
constexpr size_t WS_ACT = WS_O + (2u << 20);
constexpr size_t WS_KMAX = WS_ACT + (size_t)S * F * 2;
constexpr size_t WS_G = WS_KMAX + 32 * 64 * 4;

DI f32x16 mfma(bf16x8 a, bf16x8 b, f32x16 c) { return __builtin_amdgcn_mfma_f32_32x32x16_bf16(a, b, c, 0, 0, 0); }
DI unsigned pk(float a, float b) { f32x2 f = {a, b}; return __builtin_bit_cast(unsigned, __builtin_convertvector(f, bf16x2)); }
DI float bflo(unsigned u) { return __uint_as_float(u << 16); }
DI float bfhi(unsigned u) { return __uint_as_float(u & 0xffff0000u); }
DI void glds16(const void* gsrc, unsigned lds_dst) {
    unsigned keep;
    asm volatile("s_mov_b32 %0, m0\n\ts_mov_b32 m0, %2\n\ts_nop 0\n\tglobal_load_lds_dwordx4 %1, off\n\ts_mov_b32 m0, %0"
                 : "=&s"(keep) : "v"(gsrc), "s"(lds_dst) : "memory");
}
#define WAITV(n) asm volatile("s_waitcnt vmcnt(" #n ")" ::: "memory")
DI void barrier() { asm volatile("s_waitcnt lgkmcnt(0)\n\ts_barrier" ::: "memory"); }
DI bf16x8 lds128(unsigned addr) { return *(const LAS bf16x8*)(uintptr_t)addr; }
DI bf16x8 ldstr(unsigned a0, unsigned a1) {
    s16x4 lo = __builtin_amdgcn_ds_read_tr16_b64_v4i16((LAS s16x4*)(uintptr_t)a0);
    s16x4 hi = __builtin_amdgcn_ds_read_tr16_b64_v4i16((LAS s16x4*)(uintptr_t)a1);
    typedef short s16x8 __attribute__((ext_vector_type(8)));
    s16x8 r = __builtin_shufflevector(lo, hi, 0, 1, 2, 3, 4, 5, 6, 7);
    return __builtin_bit_cast(bf16x8, r);
}
DI float swap_half(float v) {
    return __shfl_xor(v, 32, 64);
}

__global__ __launch_bounds__(512) void k1_gemm(const __bf16* __restrict__ x, const __bf16* __restrict__ wqkv, const __bf16* __restrict__ w1,
                                                const __bf16* __restrict__ b1, char* __restrict__ ws) {
    extern __shared__ __attribute__((aligned(16))) char smem[];
    const unsigned sbase = (unsigned)(uintptr_t)(LAS char*)smem;
    const int lane = threadIdx.x & 63, w = __builtin_amdgcn_readfirstlane(threadIdx.x >> 6);
    const int bid = blockIdx.x;
    const int rb = bid & 15, cbk = bid >> 4;
    const bool isqkv = cbk < 12;
    const __bf16* W = isqkv ? wqkv + cbk * 128 : w1 + (cbk - 12) * 128;
    const int ldw = isqkv ? 1536 : F;
    const __bf16* X = x + (size_t)rb * 128 * D;
    const int lr = lane >> 3, lp = lane & 7;
    const char* xs[2]; const char* wsx[2];
#pragma unroll
    for (int i = 0; i < 2; i++) {
        int row = w * 16 + i * 8 + lr;
        xs[i] = (const char*)(X + (size_t)row * D) + ((lp ^ ((row >> 1) & 7)) << 4);
        int kr = (w >> 1) * 16 + i * 8 + lr;
        wsx[i] = (const char*)(W + (size_t)kr * ldw + (w & 1) * 64) + ((lp ^ (((kr >> 1) & 1) << 2)) << 4);
    }
    auto stage = [&](int s) {
        unsigned slot = sbase + (s & 3) * 32768;
#pragma unroll
        for (int i = 0; i < 2; i++) {
            glds16(xs[i] + s * 128, slot + (w * 16 + i * 8) * 128);
            glds16(wsx[i] + (size_t)s * 64 * ldw * 2, slot + 16384 + (w & 1) * 8192 + ((w >> 1) * 16 + i * 8) * 128);
        }
    };
    stage(0); stage(1); stage(2); stage(3);
    const int wr = w >> 1, wc = w & 1;
    const int r = lane & 31, h = lane >> 5;
    const bool isv = isqkv && cbk >= 8;
    f32x16 acc[2];
#pragma unroll
    for (int c = 0; c < 2; c++)
#pragma unroll
        for (int i = 0; i < 16; i++) acc[c][i] = 0.f;
    const unsigned xrow = (wr * 32 + r) * 128;
    const int xf = (r >> 1) & 7;
    const int q = (lane & 15) >> 2, p = lane & 3, blk = (lane >> 4) & 1;
    const unsigned wrow = 16384 + wc * 8192 + (8 * h + q) * 128 + blk * 32 + p * 8;
    const unsigned wsw = ((q >> 1) & 1) * 64;
#pragma unroll
    for (int s = 0; s < 8; s++) {
        if (s == 0) WAITV(12); else if (s < 6) WAITV(8); else if (s == 6) WAITV(4); else WAITV(0);
        barrier();
        if (s >= 1 && s + 3 < 8) stage(s + 3);
        unsigned slot = sbase + (s & 3) * 32768;
#pragma unroll
        for (int ks = 0; ks < 4; ks++) {
            bf16x8 xa = lds128(slot + xrow + (((2 * ks + h) ^ xf) << 4));
#pragma unroll
            for (int c = 0; c < 2; c++) {
                unsigned a = slot + wrow + ks * 2048 + ((c * 64) ^ wsw);
                bf16x8 wb = ldstr(a, a + 512);
                if (isv) acc[c] = mfma(xa, wb, acc[c]); else acc[c] = mfma(wb, xa, acc[c]);
            }
        }
    }
    const int rt = rb * 4 + wr;
    if (isqkv) {
        const int mat = cbk >> 2;
#pragma unroll
        for (int c = 0; c < 2; c++) {
            const int head0 = ((cbk & 3) * 128 + wc * 64 + c * 32) >> 4;
            if (mat < 2) {
                const float sc = mat == 0 ? 0.25f * LOG2E : 1.0f;
#pragma unroll
                for (int hh = 0; hh < 2; hh++) {
                    u32x4 o;
#pragma unroll
                    for (int j = 0; j < 4; j++) o[j] = pk(acc[c][hh * 8 + 2 * j] * sc, acc[c][hh * 8 + 2 * j + 1] * sc);
                    char* dst = ws + (mat == 0 ? WS_Q : WS_K) + ((size_t)((head0 + hh) * 64 + rt) * 64 + lane) * 16;
                    *(u32x4*)dst = o;
                    if (mat == 1) {
                        float n2 = 0.f;
#pragma unroll
                        for (int j = 0; j < 4; j++) { float a = bflo(o[j]), b = bfhi(o[j]); n2 += a * a + b * b; }
                        n2 += swap_half(n2);
#pragma unroll
                        for (int m = 1; m < 32; m <<= 1) n2 = fmaxf(n2, __shfl_xor(n2, m, 64));
                        if (lane == 0) ((float*)(ws + WS_KMAX))[(head0 + hh) * 64 + rt] = n2;
                    }
                }
            } else {
#pragma unroll
                for (int s2 = 0; s2 < 2; s2++) {
                    u32x4 o;
#pragma unroll
                    for (int j = 0; j < 4; j++) o[j] = pk(acc[c][s2 * 8 + 2 * j], acc[c][s2 * 8 + 2 * j + 1]);
                    char* dst = ws + WS_V + (size_t)((head0 + (r >> 4)) * 64 + rt) * 1024 + s2 * 512 + (h * 16 + (r & 15)) * 16;
                    *(u32x4*)dst = o;
                }
            }
        }
    } else {
#pragma unroll
        for (int c = 0; c < 2; c++) {
            const int f0 = (cbk - 12) * 128 + wc * 64 + c * 32;
#pragma unroll
            for (int s2 = 0; s2 < 2; s2++) {
                float y[8];
#pragma unroll
                for (int j = 0; j < 8; j++) {
                    int f = f0 + 16 * s2 + 8 * (j >> 2) + 4 * h + (j & 3);
                    float hv = acc[c][s2 * 8 + j] + (float)b1[f];
                    float e = __builtin_amdgcn_exp2f(__builtin_fmaf(hv, LOG2E, -0.5f));
                    float v = e + 0.70710678118654752f;
                    float rr = __builtin_amdgcn_rcpf(__builtin_fmaf(v, v, 0.5f));
                    y[j] = __builtin_fmaf(-hv, rr, hv);
                }
                u32x4 o;
#pragma unroll
                for (int j = 0; j < 4; j++) o[j] = pk(y[2 * j], y[2 * j + 1]);
                char* dst = ws + WS_ACT + ((size_t)(rt * 112 + (f0 >> 4) + s2) * 64 + lane) * 16;
                *(u32x4*)dst = o;
            }
        }
    }
}

constexpr unsigned L_K = 0, L_V = 65536, L_ONE = 131072, ATT_LDS = 131072 + 4096;

__global__ __launch_bounds__(512) void k2_attn(const __bf16* __restrict__ x, const __bf16* __restrict__ gate_w, char* __restrict__ ws) {
    extern __shared__ __attribute__((aligned(16))) char smem[];
    const unsigned sbase = (unsigned)(uintptr_t)(LAS char*)smem;
    const int lane = threadIdx.x & 63, w = __builtin_amdgcn_readfirstlane(threadIdx.x >> 6);
    const int bid = blockIdx.x;
    const int head = (bid & 7) * 4 + ((bid >> 3) & 3), qb = bid >> 5;
    const int r = lane & 31, h = lane >> 5;
    const char* kg = ws + WS_K + (size_t)head * 65536 + lane * 16;
    const char* vg = ws + WS_V + (size_t)head * 65536 + lane * 16;
    const int qt = qb * 8 + w;
    u32x4 qraw = *(const u32x4*)(ws + WS_Q + ((size_t)(head * 64 + qt) * 64 + lane) * 16);
    float km = ((const float*)(ws + WS_KMAX))[head * 64 + lane];
    u32x4 xg = *(const u32x4*)(x + (size_t)(bid * 8 + w) * D + lane * 8);
    u32x4 gw = *(const u32x4*)(gate_w + lane * 8);
    asm volatile("" ::: "memory");
#pragma unroll
    for (int c = 0; c < 8; c++) {
        int t = c * 8 + w;
        glds16(kg + t * 1024, sbase + L_K + t * 1024);
        glds16(vg + t * 1024, sbase + L_V + t * 1024);
    }
    if (threadIdx.x < 256) *(LAS u32x4*)(uintptr_t)(sbase + L_ONE + threadIdx.x * 16) = (u32x4){0x3f803f80u, 0x3f803f80u, 0x3f803f80u, 0x3f803f80u};
    {
        float g = 0.f;
#pragma unroll
        for (int j = 0; j < 4; j++) g += bflo(xg[j]) * bflo(gw[j]) + bfhi(xg[j]) * bfhi(gw[j]);
#pragma unroll
        for (int m = 1; m < 64; m <<= 1) g += __shfl_xor(g, m, 64);
        if (lane == 0) ((float*)(ws + WS_G))[bid * 8 + w] = 1.0f / (1.0f + __expf(-g));
    }
#pragma unroll
    for (int m = 1; m < 64; m <<= 1) km = fmaxf(km, __shfl_xor(km, m, 64));
    float q2 = 0.f;
#pragma unroll
    for (int j = 0; j < 4; j++) { float a = bflo(qraw[j]), b = bfhi(qraw[j]); q2 += a * a + b * b; }
    q2 += swap_half(q2);
    float mrow = sqrtf(q2 * km);
    const bf16x8 qf = __builtin_bit_cast(bf16x8, qraw);
    WAITV(0);
    barrier();

    const unsigned kaddr0 = sbase + L_K + lane * 16;
    const unsigned vaddr0 = r < 16 ? sbase + L_V + (h * 16 + r) * 16 : sbase + L_ONE + ((r >= 20 && r < 28) ? 64 : 0);
    const unsigned vstep = r < 16 ? 4096u : 0u;
    f32x16 o;
    for (int attempt = 0; attempt < 2; attempt++) {
        f32x16 negm;
#pragma unroll
        for (int i = 0; i < 16; i++) { negm[i] = -mrow; o[i] = 0.f; }
        unsigned ka = kaddr0, va = vaddr0;
        for (int tt = 0; tt < 16; tt++) {
#pragma unroll
            for (int t = 0; t < 4; t++) {
                bf16x8 kf = lds128(ka + t * 1024);
                bf16x8 v0 = lds128(va + t * 1024);
                bf16x8 v1 = lds128(va + t * 1024 + 512);
                f32x16 s = mfma(kf, qf, negm);
                u32x4 p0, p1;
#pragma unroll
                for (int j = 0; j < 4; j++) {
                    p0[j] = pk(__builtin_amdgcn_exp2f(s[2 * j]), __builtin_amdgcn_exp2f(s[2 * j + 1]));
                    p1[j] = pk(__builtin_amdgcn_exp2f(s[8 + 2 * j]), __builtin_amdgcn_exp2f(s[8 + 2 * j + 1]));
                }
                o = mfma(v0, __builtin_bit_cast(bf16x8, p0), o);
                o = mfma(v1, __builtin_bit_cast(bf16x8, p1), o);
            }
            ka += 4096; va += vstep;
        }
        const bool bad = !(o[8] >= 0x1p-60f);
        if (attempt == 1 || !__any(bad)) break;
        f32x16 z;
#pragma unroll
        for (int i = 0; i < 16; i++) z[i] = 0.f;
        float mx = -INFINITY;
        for (int t = 0; t < 64; t++) {
            f32x16 s = mfma(lds128(kaddr0 + t * 1024), qf, z);
#pragma unroll
            for (int i = 0; i < 16; i++) mx = fmaxf(mx, s[i]);
        }
        mrow = fmaxf(mx, swap_half(mx));
    }
    const float inv = 1.0f / o[8];
    u32x4 ov;
#pragma unroll
    for (int j = 0; j < 4; j++) ov[j] = pk(o[2 * j] * inv, o[2 * j + 1] * inv);
    *(u32x4*)(ws + WS_O + ((size_t)(qt * 32 + head) * 64 + lane) * 16) = ov;
}

__global__ __launch_bounds__(256) void k3_out(const __bf16* __restrict__ wo, const __bf16* __restrict__ w2, const char* __restrict__ ws,
                                               __bf16* __restrict__ out) {
    extern __shared__ __attribute__((aligned(16))) char smem[];
    const unsigned sbase = (unsigned)(uintptr_t)(LAS char*)smem;
    const int lane = threadIdx.x & 63, w = __builtin_amdgcn_readfirstlane(threadIdx.x >> 6);
    const int bid = blockIdx.x;
    const int rb = (bid & 7) * 4 + ((bid >> 3) & 3), cb = bid >> 5;
    const int r = lane & 31, h = lane >> 5;
    const int wr = w >> 1, wc = w & 1;
    const int rt = rb * 2 + wr;
    const int lr = lane >> 3, lp = lane & 7;
    unsigned woff[2];
#pragma unroll
    for (int i = 0; i < 2; i++) { int kr = w * 16 + i * 8 + lr; woff[i] = kr * 1024 + cb * 128 + ((lp ^ (((kr >> 1) & 1) << 2)) << 4); }
    auto stage = [&](int s) {
        const char* base = s < 8 ? (const char*)wo + (size_t)s * 65536 : (const char*)w2 + (size_t)(s - 8) * 65536;
#pragma unroll
        for (int i = 0; i < 2; i++) glds16(base + woff[i], sbase + (s & 7) * 8192 + (w * 16 + i * 8) * 128);
    };
    const char* of = ws + WS_O + ((size_t)rt * 32 * 64 + lane) * 16;
    const char* af = ws + WS_ACT + ((size_t)rt * 112 * 64 + lane) * 16;
    const int q = (lane & 15) >> 2, p = lane & 3, blk = (lane >> 4) & 1;
    const unsigned wrow = (4 * h + q) * 128 + blk * 32 + p * 8 + ((wc * 64) ^ (((q >> 1) & 1) * 64));
    f32x16 acca, accb;
#pragma unroll
    for (int i = 0; i < 16; i++) { acca[i] = 0.f; accb[i] = 0.f; }
    for (int s = 0; s < 36; s++) {
        stage(s);
        bf16x8 a[4];
        const char* src = s < 8 ? of + s * 4096 : af + (s - 8) * 4096;
#pragma unroll
        for (int ks = 0; ks < 4; ks++) a[ks] = *(const bf16x8*)(src + ks * 1024);
        WAITV(0);
        barrier();
        unsigned slot = sbase + (s & 7) * 8192;
#pragma unroll
        for (int ks = 0; ks < 4; ks++) {
            unsigned ad = slot + wrow + ks * 2048;
            bf16x8 wb = ldstr(ad, ad + 1024);
            if (s < 8) acca = mfma(wb, a[ks], acca); else accb = mfma(wb, a[ks], accb);
        }
    }
    const int tok = rt * 32 + r;
    const float g = ((const float*)(ws + WS_G))[tok];
#pragma unroll
    for (int gq = 0; gq < 4; gq++) {
        u32x2 o;
        o[0] = pk(g * acca[4 * gq] + (1.f - g) * accb[4 * gq], g * acca[4 * gq + 1] + (1.f - g) * accb[4 * gq + 1]);
        o[1] = pk(g * acca[4 * gq + 2] + (1.f - g) * accb[4 * gq + 2], g * acca[4 * gq + 3] + (1.f - g) * accb[4 * gq + 3]);
        *(u32x2*)(out + (size_t)tok * D + cb * 64 + wc * 32 + 8 * gq + 4 * h) = o;
    }
}

extern "C" void kernel_launch(void* const* d_in, const int* in_sizes, int n_in, void* d_out, int out_size, void* d_ws, size_t ws_size,
                              hipStream_t stream) {
    const __bf16* x = (const __bf16*)d_in[0];
    const __bf16* wqkv = (const __bf16*)d_in[1];
    const __bf16* wo = (const __bf16*)d_in[2];
    const __bf16* w1 = (const __bf16*)d_in[4];
    const __bf16* b1 = (const __bf16*)d_in[5];
    const __bf16* w2 = (const __bf16*)d_in[6];
    const __bf16* gw = (const __bf16*)d_in[7];
    char* ws = (char*)d_ws;
    (void)hipFuncSetAttribute((const void*)k1_gemm, hipFuncAttributeMaxDynamicSharedMemorySize, 131072);
    (void)hipFuncSetAttribute((const void*)k2_attn, hipFuncAttributeMaxDynamicSharedMemorySize, ATT_LDS);
    (void)hipFuncSetAttribute((const void*)k3_out, hipFuncAttributeMaxDynamicSharedMemorySize, 65536);
    k1_gemm<<<416, 512, 131072, stream>>>(x, wqkv, w1, b1, ws);
    k2_attn<<<256, 512, ATT_LDS, stream>>>(x, gw, ws);
    k3_out<<<256, 256, 65536, stream>>>(wo, w2, ws, (__bf16*)d_out);
}
```

```cpp
#include <hip/hip_runtime.h>
#include <hip/hip_bf16.h>
#include <stdint.h>

typedef __bf16 bf16x8 __attribute__((ext_vector_type(8)));
typedef __bf16 bf16x2 __attribute__((ext_vector_type(2)));
typedef short s16x4 __attribute__((ext_vector_type(4)));
typedef short s16x8 __attribute__((ext_vector_type(8)));
typedef float f32x2 __attribute__((ext_vector_type(2)));
typedef float f32x4 __attribute__((ext_vector_type(4)));
typedef float f32x16 __attribute__((ext_vector_type(16)));
typedef unsigned u32x4 __attribute__((ext_vector_type(4)));
typedef unsigned u32x2 __attribute__((ext_vector_type(2)));
#define LAS __attribute__((address_space(3)))
#define GAS __attribute__((address_space(1)))
#define DI __device__ __forceinline__

constexpr int S = 2048, D = 512, F = 1792;
constexpr float LOG2E = 1.4426950408889634f;

constexpr size_t WS_Q = 0;
constexpr size_t WS_K = WS_Q + (2u << 20);
constexpr size_t WS_V = WS_K + (2u << 20);
constexpr size_t WS_O = WS_V + (2u << 20);
constexpr size_t WS_ACT = WS_O + (2u << 20);
constexpr size_t WS_KMAX = WS_ACT + (size_t)S * F * 2;
constexpr size_t WS_G = WS_KMAX + 32 * 64 * 4;
constexpr size_t WS_FLAG = WS_G + 2048 * 4;

#define STAMPW(k, s, wv) do {} while (0)

DI f32x16 mfma(bf16x8 a, bf16x8 b, f32x16 c) { return __builtin_amdgcn_mfma_f32_32x32x16_bf16(a, b, c, 0, 0, 0); }
DI unsigned pk(float a, float b) { f32x2 f = {a, b}; return __builtin_bit_cast(unsigned, __builtin_convertvector(f, bf16x2)); }
DI float bflo(unsigned u) { return __uint_as_float(u << 16); }
DI float bfhi(unsigned u) { return __uint_as_float(u & 0xffff0000u); }
DI void glds16(const void* sbase, unsigned voff, unsigned lds_dst) {
    unsigned keep;
    asm volatile("s_mov_b32 %0, m0\n\ts_mov_b32 m0, %3\n\ts_nop 0\n\tglobal_load_lds_dwordx4 %1, %2\n\ts_mov_b32 m0, %0"
                 : "=&s"(keep) : "v"(voff), "s"(sbase), "s"(lds_dst) : "memory");
}
DI const char* uni(const void* p) {
    unsigned long long v = (unsigned long long)p;
    unsigned lo = __builtin_amdgcn_readfirstlane((unsigned)v), hi = __builtin_amdgcn_readfirstlane((unsigned)(v >> 32));
    return (const char*)(((unsigned long long)hi << 32) | lo);
}
#define WAITV(n) asm volatile("s_waitcnt vmcnt(" #n ")" ::: "memory")
DI void barrier() { asm volatile("s_waitcnt lgkmcnt(0)\n\ts_barrier" ::: "memory"); }
DI bf16x8 lds128(unsigned addr) { return *(const LAS bf16x8*)(uintptr_t)addr; }
DI bf16x8 ldstr(unsigned a0, unsigned a1) {
    s16x4 lo = __builtin_amdgcn_ds_read_tr16_b64_v4i16((LAS s16x4*)(uintptr_t)a0);
    s16x4 hi = __builtin_amdgcn_ds_read_tr16_b64_v4i16((LAS s16x4*)(uintptr_t)a1);
    s16x8 r = __builtin_shufflevector(lo, hi, 0, 1, 2, 3, 4, 5, 6, 7);
    return __builtin_bit_cast(bf16x8, r);
}
DI float swap_half(float v) {
    u32x2 r = __builtin_amdgcn_permlane32_swap(__float_as_uint(v), __float_as_uint(v), false, false);
    return __uint_as_float((threadIdx.x & 32) ? r[0] : r[1]);
}
template <int CTRL> DI float dppf(float v) { return __uint_as_float(__builtin_amdgcn_update_dpp(0, __float_as_uint(v), CTRL, 0xf, 0xf, true)); }
DI float row_sum16(float v) { v += dppf<0xB1>(v); v += dppf<0x4E>(v); v += dppf<0x141>(v); v += dppf<0x140>(v); return v; }
DI float row_max16(float v) { v = fmaxf(v, dppf<0xB1>(v)); v = fmaxf(v, dppf<0x4E>(v)); v = fmaxf(v, dppf<0x141>(v)); v = fmaxf(v, dppf<0x140>(v)); return v; }
DI float rdlane(float v, int l) { return __uint_as_float(__builtin_amdgcn_readlane(__float_as_uint(v), l)); }
DI float wave_sum(float v) { v = row_sum16(v); return (rdlane(v, 0) + rdlane(v, 16)) + (rdlane(v, 32) + rdlane(v, 48)); }
DI float wave_max(float v) { v = row_max16(v); return fmaxf(fmaxf(rdlane(v, 0), rdlane(v, 16)), fmaxf(rdlane(v, 32), rdlane(v, 48))); }

constexpr int K1_LDS = 5 * 32768;

DI float mish(float hv) {
    float e = __builtin_amdgcn_exp2f(__builtin_fmaf(hv, LOG2E, -0.5f));
    float v = e + 0.70710678118654752f;
    float r = __builtin_amdgcn_rcpf(__builtin_fmaf(v, v, 0.5f));
    return __builtin_fmaf(-hv, r, hv);
}

typedef __amdgpu_buffer_rsrc_t rsrc_t;
DI rsrc_t mkrsrc(const void* p, unsigned bytes) { return __builtin_amdgcn_make_buffer_rsrc((void*)p, 0, bytes, 0x00020000); }
DI void st16(rsrc_t rs, unsigned off, u32x4 v) { __builtin_amdgcn_raw_buffer_store_b128(v, rs, off, 0, 16); }
DI void st4(rsrc_t rs, unsigned off, unsigned v) { __builtin_amdgcn_raw_buffer_store_b32(v, rs, off, 0, 16); }

__global__ __launch_bounds__(512, 2) void k1_proj(const __bf16* __restrict__ x, const __bf16* __restrict__ wqkv, const __bf16* __restrict__ w1,
                                                   const __bf16* __restrict__ b1, const __bf16* __restrict__ gate_w, char* __restrict__ ws) {
    extern __shared__ __attribute__((aligned(16))) char smem[];
    const unsigned sbase = (unsigned)(uintptr_t)(LAS char*)smem;
    const int lane = threadIdx.x & 63, w = __builtin_amdgcn_readfirstlane(threadIdx.x >> 6);
    const int bid = blockIdx.x;
    STAMPW(0, 0, 0);
    const int xg = bid & 7, li = bid >> 3;
    const int rb = (xg >> 2) * 16 + (li & 15);
    const int cb = (xg & 3) + (li >> 4) * 4;
    const int rot = (li + (li >> 3)) & 7;
    const bool nu4 = cb < 4;
    const int u0 = nu4 ? cb * 4 : 16 + (cb - 4) * 3;

    if (w >= 4) {
        const int lw = w - 4;
        const int prow = lw * 16 + (lane >> 3), lp = lane & 7;
        const unsigned xoff = (unsigned)((rb * 64 + prow) * 1024 + ((lp ^ ((prow >> 1) & 7)) << 4));
        const unsigned xoff2 = (unsigned)((rb * 64 + prow + 8) * 1024 + ((lp ^ (((prow + 8) >> 1) & 7)) << 4));
        const unsigned wsw16 = (unsigned)((lp ^ (((prow >> 1) & 1) << 2)) << 4);
        const unsigned qoff = (unsigned)(prow * 3072 + cb * 128) + wsw16;
        const unsigned foff = (unsigned)(prow * (F * 2) + u0 * 128) + wsw16;
        auto stage = [&](int s) {
            const unsigned dst = sbase + (unsigned)(s % 5) * 32768u + lw * 2048;
            const int kc = ((s & 7) + rot) & 7;
            if (s < 8) {
                glds16((const char*)x + kc * 128, xoff, dst);
                glds16((const char*)x + kc * 128, xoff2, dst + 1024);
                const char* wb = (const char*)wqkv + kc * (64 * 3072);
#pragma unroll
                for (int m = 0; m < 3; m++) {
                    glds16(wb + m * 1024, qoff, dst + 8192 * (m + 1));
                    glds16(wb + m * 1024 + 8 * 3072, qoff, dst + 8192 * (m + 1) + 1024);
                }
            } else {
                const char* wb = (const char*)w1 + kc * (64 * F * 2);
#pragma unroll
                for (int m = 0; m < 4; m++) {
                    if (m < 3 || nu4) {
                        glds16(wb + m * 128, foff, dst + 8192 * m);
                        glds16(wb + m * 128 + 8 * F * 2, foff, dst + 8192 * m + 1024);
                    }
                }
            }
        };
        stage(0); stage(1); stage(2);
        WAITV(8);
        barrier();
        stage(3); stage(4);
#pragma unroll
        for (int s = 1; s < 15; s++) {
            if (s < 5) WAITV(16);
            else if (s < 13) { if (nu4) WAITV(16); else if (s == 5) WAITV(14); else WAITV(12); }
            else if (s == 13) { if (nu4) WAITV(8); else WAITV(6); }
            else WAITV(0);
            barrier();
            if (s + 4 < 16) stage(s + 4);
        }
        return;
    }
    const int rt = w & 1, cg = w >> 1;
    const int r = lane & 31, h = lane >> 5;
    const rsrc_t wsr = mkrsrc(ws, 32u << 20);
    u32x2 bias[4][4];
#pragma unroll
    for (int t = 0; t < 4; t++)
#pragma unroll
        for (int g = 0; g < 4; g++) bias[t][g] = *(const u32x2*)(b1 + (u0 + ((t < 3 || nu4) ? t : 0)) * 64 + cg * 32 + 8 * g + 4 * h);
    {
        const u32x4 gw4 = *(const u32x4*)(gate_w + lane * 8);
#pragma unroll
        for (int i2 = 0; i2 < 2; i2++) {
            const u32x4 xg4 = *(const u32x4*)(x + (size_t)(bid * 8 + 2 * w + i2) * D + lane * 8);
            float g = 0.f;
#pragma unroll
            for (int i = 0; i < 4; i++) g += bflo(xg4[i]) * bflo(gw4[i]) + bfhi(xg4[i]) * bfhi(gw4[i]);
            g = wave_sum(g);
            if (lane == 0) st4(wsr, (unsigned)WS_G + (bid * 8 + 2 * w + i2) * 4, __float_as_uint(__builtin_amdgcn_rcpf(1.0f + __builtin_amdgcn_exp2f(-LOG2E * g))));
        }
        if (w == 0 && lane < 8) st4(wsr, (unsigned)WS_FLAG + (bid * 8 + lane) * 4, 0u);
    }
    const unsigned xrow = sbase + (unsigned)((rt * 32 + r) * 128);
    const int xf = (r >> 1) & 7;
    const int q = (lane & 15) >> 2, p = lane & 3, blk = (lane >> 4) & 1;
    const unsigned wrd = sbase + (unsigned)((8 * h + q) * 128 + blk * 32 + p * 8) + (unsigned)((cg * 64) ^ (((q >> 1) & 1) * 64));
    bf16x8 xa[32];
    bf16x8 wf[2][4];
    f32x16 acc[4];
#pragma unroll
    for (int t = 0; t < 3; t++)
#pragma unroll
        for (int i = 0; i < 16; i++) acc[t][i] = 0.f;
    auto fetch = [&](int st) {
        const int s = st >> 2, ks = st & 3;
        const unsigned so = (unsigned)(s % 5) * 32768u;
        if (s < 8) {
            xa[st] = lds128(xrow + so + (unsigned)(((2 * ks + h) ^ xf) << 4));
#pragma unroll
            for (int m = 0; m < 3; m++) { const unsigned a = wrd + so + ks * 2048 + 8192 * (m + 1); wf[st & 1][m] = ldstr(a, a + 512); }
        } else {
#pragma unroll
            for (int m = 0; m < 4; m++) { const unsigned a = wrd + so + ks * 2048 + 8192 * m; wf[st & 1][m] = ldstr(a, a + 512); }
        }
    };
    barrier();
    STAMPW(3, 0, 0);
    fetch(0);
#pragma unroll
    for (int st = 0; st < 32; st++) {
        if ((st & 3) == 0 && st > 0) { barrier(); STAMPW(3, st >> 2, 0); }
        fetch(st + 1);
        acc[0] = mfma(wf[st & 1][0], xa[st], acc[0]);
        acc[1] = mfma(wf[st & 1][1], xa[st], acc[1]);
        acc[2] = mfma(xa[st], wf[st & 1][2], acc[2]);
    }
    STAMPW(0, 1, 0);
    const int rtile = rb * 2 + rt;
    {
        const int head0 = cb * 4 + cg * 2;
        const int slot_r = (r & 19) | ((r & 4) << 1) | ((r & 8) >> 1);
#pragma unroll
        for (int hh = 0; hh < 2; hh++) {
            u32x4 o;
            float n2 = 0.f;
#pragma unroll
            for (int i = 0; i < 4; i++) {
                o[i] = pk(acc[1][hh * 8 + 2 * i], acc[1][hh * 8 + 2 * i + 1]);
                float a = bflo(o[i]), b = bfhi(o[i]);
                n2 += a * a + b * b;
            }
            st16(wsr, (unsigned)WS_K + (((head0 + hh) * 64 + rtile) * 64 + h * 32 + slot_r) * 16, o);
#pragma unroll
            for (int i = 0; i < 4; i++) o[i] = pk(acc[2][hh * 8 + 2 * i], acc[2][hh * 8 + 2 * i + 1]);
            st16(wsr, (unsigned)WS_V + ((head0 + (r >> 4)) * 64 + rtile) * 1024 + hh * 512 + (h * 16 + (r & 15)) * 16, o);
#pragma unroll
            for (int i = 0; i < 4; i++) o[i] = pk(acc[0][hh * 8 + 2 * i] * (0.25f * LOG2E), acc[0][hh * 8 + 2 * i + 1] * (0.25f * LOG2E));
            st16(wsr, (unsigned)WS_Q + (((head0 + hh) * 64 + rtile) * 64 + lane) * 16, o);
            n2 += swap_half(n2);
            n2 = row_max16(n2);
            n2 = fmaxf(rdlane(n2, 0), rdlane(n2, 16));
            if (lane == 0) st4(wsr, (unsigned)WS_KMAX + ((head0 + hh) * 64 + rtile) * 4, __float_as_uint(n2));
        }
    }
#pragma unroll
    for (int t = 0; t < 4; t++)
#pragma unroll
        for (int g = 0; g < 4; g++) {
            acc[t][4 * g] = bflo(bias[t][g][0]); acc[t][4 * g + 1] = bfhi(bias[t][g][0]);
            acc[t][4 * g + 2] = bflo(bias[t][g][1]); acc[t][4 * g + 3] = bfhi(bias[t][g][1]);
        }
#pragma unroll
    for (int st = 32; st < 64; st++) {
        if ((st & 3) == 0 && st < 60) barrier();
        if (st + 1 < 64) fetch(st + 1);
        acc[0] = mfma(wf[st & 1][0], xa[st - 32], acc[0]);
        acc[1] = mfma(wf[st & 1][1], xa[st - 32], acc[1]);
        acc[2] = mfma(wf[st & 1][2], xa[st - 32], acc[2]);
        if (nu4) acc[3] = mfma(wf[st & 1][3], xa[st - 32], acc[3]);
    }
    STAMPW(0, 2, 0);
    {
        const unsigned dst = (unsigned)WS_ACT + ((rtile * 112 + u0 * 4 + cg * 2) * 64 + lane) * 16;
#pragma unroll
        for (int t = 0; t < 4; t++) {
            if (t < 3 || nu4) {
#pragma unroll
                for (int s2 = 0; s2 < 2; s2++) {
                    u32x4 o;
#pragma unroll
                    for (int i = 0; i < 4; i++) o[i] = pk(mish(acc[t][s2 * 8 + 2 * i]), mish(acc[t][s2 * 8 + 2 * i + 1]));
                    st16(wsr, dst + t * 4096 + s2 * 1024, o);
                }
            }
        }
    }
    STAMPW(0, 3, 0);
}

constexpr unsigned L_K = 0, L_V = 65536, L_ONE = 131072, K2_LDS = 163840;
#define AG16(b) "a" #b "0","a" #b "1","a" #b "2","a" #b "3","a" #b "4","a" #b "5","a" #b "6","a" #b "7","a" #b "8","a" #b "9"
#define ACLOB "a0","a1","a2","a3","a4","a5","a6","a7","a8","a9", AG16(1), AG16(2), AG16(3), AG16(4), AG16(5), AG16(6), AG16(7), AG16(8), AG16(9), AG16(10), \
    "a110","a111","a112"

template <int N> DI void lda(const void* base, unsigned voff) {
    asm volatile("global_load_dwordx4 a[%c2:%c2+3], %0, %1 offset:%c3" :: "v"(voff), "s"(base), "i"(4 * N), "i"((N & 3) * 1024) : "memory");
}
template <int N> DI void lda_sc1(const void* base, unsigned voff) {
    asm volatile("global_load_dwordx4 a[%c2:%c2+3], %0, %1 offset:%c3 sc1" :: "v"(voff), "s"(base), "i"(4 * N), "i"((N & 3) * 1024) : "memory");
}
typedef float f32x8 __attribute__((ext_vector_type(8)));
DI f32x16 mfma_v(bf16x8 a, bf16x8 b, f32x16 c) {
    f32x16 d;
    asm volatile("s_nop 1\n\tv_mfma_f32_32x32x16_bf16 %0, %1, %2, %3\n\ts_nop 7\n\ts_nop 4" : "=&v"(d) : "v"(a), "v"(b), "v"(c));
    return d;
}
DI void smfmac_v(f32x16& o, u32x4 a, f32x8 p, unsigned idx) {
    asm volatile("s_nop 1\n\tv_smfmac_f32_32x32x32_bf16 %0, %1, %2, %3\n\ts_nop 7\n\ts_nop 4" : "+v"(o) : "v"(a), "v"(p), "v"(idx));
}
DI int piece_slot(int n) { return n < 3 ? 17 + n : (n == 19 ? 16 : (((n - 3) & 1) << 3) + ((n - 3) >> 1)); }

template <int A0> DI void round_mma(f32x16& c0, f32x16& c1, unsigned a0, unsigned a1) {
    asm volatile(R"(
    ds_read_b64_tr_b16 v[64:65], %2
    ds_read_b64_tr_b16 v[66:67], %2 offset:1024
    ds_read_b64_tr_b16 v[68:69], %3
    ds_read_b64_tr_b16 v[70:71], %3 offset:1024
    ds_read_b64_tr_b16 v[72:73], %2 offset:2048
    ds_read_b64_tr_b16 v[74:75], %2 offset:3072
    ds_read_b64_tr_b16 v[76:77], %3 offset:2048
    ds_read_b64_tr_b16 v[78:79], %3 offset:3072
    ds_read_b64_tr_b16 v[80:81], %2 offset:4096
    ds_read_b64_tr_b16 v[82:83], %2 offset:5120
    ds_read_b64_tr_b16 v[84:85], %3 offset:4096
    ds_read_b64_tr_b16 v[86:87], %3 offset:5120
    s_waitcnt lgkmcnt(10)
    v_mfma_f32_32x32x16_bf16 %0, v[64:67], a[%c4:%c4+3], %0
    ds_read_b64_tr_b16 v[88:89], %2 offset:6144
    ds_read_b64_tr_b16 v[90:91], %2 offset:7168
    s_waitcnt lgkmcnt(10)
    v_mfma_f32_32x32x16_bf16 %1, v[68:71], a[%c4:%c4+3], %1
    ds_read_b64_tr_b16 v[92:93], %3 offset:6144
    ds_read_b64_tr_b16 v[94:95], %3 offset:7168
    s_waitcnt lgkmcnt(10)
    v_mfma_f32_32x32x16_bf16 %0, v[72:75], a[%c4+4:%c4+7], %0
    s_waitcnt lgkmcnt(8)
    v_mfma_f32_32x32x16_bf16 %1, v[76:79], a[%c4+4:%c4+7], %1
    s_waitcnt lgkmcnt(6)
    v_mfma_f32_32x32x16_bf16 %0, v[80:83], a[%c4+8:%c4+11], %0
    s_waitcnt lgkmcnt(4)
    v_mfma_f32_32x32x16_bf16 %1, v[84:87], a[%c4+8:%c4+11], %1
    s_waitcnt lgkmcnt(2)
    v_mfma_f32_32x32x16_bf16 %0, v[88:91], a[%c4+12:%c4+15], %0
    s_waitcnt lgkmcnt(0)
    v_mfma_f32_32x32x16_bf16 %1, v[92:95], a[%c4+12:%c4+15], %1
)" : "+v"(c0), "+v"(c1) : "v"(a0), "v"(a1), "i"(A0)
   : "memory", "v64","v65","v66","v67","v68","v69","v70","v71","v72","v73","v74","v75","v76","v77","v78","v79",
     "v80","v81","v82","v83","v84","v85","v86","v87","v88","v89","v90","v91","v92","v93","v94","v95");
}

__global__ __launch_bounds__(512, 2) void k2_attn(const __bf16* __restrict__ wo, const __bf16* __restrict__ w2, char* __restrict__ ws,
                                                   __bf16* __restrict__ out) {
    extern __shared__ __attribute__((aligned(16))) char smem[];
    const unsigned sbase = (unsigned)(uintptr_t)(LAS char*)smem;
    const int lane = threadIdx.x & 63, w = __builtin_amdgcn_readfirstlane(threadIdx.x >> 6);
    const int bid = blockIdx.x;
    STAMPW(1, 0, 0);
    asm volatile("" ::: ACLOB);
    const int head = (bid & 7) * 4 + ((bid >> 3) & 3), qb = bid >> 5;
    const int rb = head, cb = qb;
    const int r = lane & 31, h = lane >> 5;
    const int qt = qb * 8 + w;
    const int rt = w & 1, kq = w >> 1;
    u32x4 qraw; float km, gq, go;
    asm volatile("global_load_dwordx4 %0, %1, %2" : "=v"(qraw) : "v"((unsigned)(((head * 64 + qt) * 64 + lane) * 16)), "s"(ws + WS_Q) : "memory");
    asm volatile("global_load_dword %0, %1, %2" : "=v"(km) : "v"((unsigned)((head * 64 + lane) * 4)), "s"(ws + WS_KMAX) : "memory");
    asm volatile("global_load_dword %0, %1, %2" : "=v"(gq) : "v"((unsigned)((qt * 32 + r) * 4)), "s"(ws + WS_G) : "memory");
    asm volatile("global_load_dword %0, %1, %2" : "=v"(go) : "v"((unsigned)((rb * 64 + rt * 32 + r) * 4)), "s"(ws + WS_G) : "memory");
    const char* kg = ws + WS_K + (size_t)head * 65536;
    const char* vg = ws + WS_V + (size_t)head * 65536;
#pragma unroll
    for (int c = 0; c < 8; c++) {
        const int t = c * 8 + w;
        glds16(kg + t * 1024, lane * 16, sbase + L_K + t * 1024);
        glds16(vg + t * 1024, lane * 16, sbase + L_V + t * 1024);
    }
    {
        const unsigned one = (threadIdx.x & 31) == 0 ? 0x3f803f80u : 0u;
        *(LAS u32x4*)(uintptr_t)(sbase + L_ONE + threadIdx.x * 16) = (u32x4){one, one, one, one};
    }
    WAITV(16);
    asm volatile("" : "+v"(qraw), "+v"(km), "+v"(gq), "+v"(go));
    km = wave_max(km);
    float q2 = 0.f;
#pragma unroll
    for (int i = 0; i < 4; i++) { float a = bflo(qraw[i]), b = bfhi(qraw[i]); q2 += a * a + b * b; }
    q2 += swap_half(q2);
    const float mrow = __builtin_amdgcn_sqrtf(q2 * km);
    const bf16x8 qf = __builtin_bit_cast(bf16x8, qraw);
    WAITV(0);
    barrier();
    STAMPW(1, 1, 0);
    const int prow = w * 8 + (lane >> 3);
    const unsigned poff = (unsigned)(prow * 1024 + cb * 128 + (((lane & 7) ^ (((prow >> 1) & 1) << 2)) << 4));
    auto piece = [&](int n) {
        const int rd = n >> 2, k4 = n & 3;
        const char* src = rd < 7 ? (const char*)w2 + (7 * k4 + rd) * 65536 : (const char*)wo + (2 * k4 + rd - 7) * 65536;
        glds16(src, poff, sbase + (unsigned)piece_slot(n % 20) * 8192u + w * 1024);
    };
    piece(0); piece(1); piece(2);
    {
        const char* ab = ws + WS_ACT + (size_t)((rb * 2 + rt) * 112 + kq * 28) * 1024;
        const unsigned lo = lane * 16;
        lda<0>(ab, lo); lda<1>(ab, lo); lda<2>(ab, lo); lda<3>(ab, lo); ab += 4096;
        lda<4>(ab, lo); lda<5>(ab, lo); lda<6>(ab, lo); lda<7>(ab, lo); ab += 4096;
        lda<8>(ab, lo); lda<9>(ab, lo); lda<10>(ab, lo); lda<11>(ab, lo); ab += 4096;
        lda<12>(ab, lo); lda<13>(ab, lo); lda<14>(ab, lo); lda<15>(ab, lo); ab += 4096;
        lda<16>(ab, lo); lda<17>(ab, lo); lda<18>(ab, lo); lda<19>(ab, lo); ab += 4096;
        lda<20>(ab, lo); lda<21>(ab, lo); lda<22>(ab, lo); lda<23>(ab, lo); ab += 4096;
        lda<24>(ab, lo); lda<25>(ab, lo); lda<26>(ab, lo); lda<27>(ab, lo);
    }
    {
        const unsigned to = (unsigned)((w * 64 + lane) * 1024 + cb * 128);
        asm volatile("global_load_dword a112, %0, %1" :: "v"(to), "s"(wo) : "memory");
        asm volatile("global_load_dword a112, %0, %1" :: "v"(to), "s"(w2) : "memory");
        asm volatile("global_load_dword a112, %0, %1" :: "v"(to + 512 * 1024), "s"(w2) : "memory");
        asm volatile("global_load_dword a112, %0, %1" :: "v"(to + 1024 * 1024), "s"(w2) : "memory");
        if (w < 4) asm volatile("global_load_dword a112, %0, %1" :: "v"(to + 1536 * 1024), "s"(w2) : "memory");
    }

    f32x16 o;
    {
        f32x16 negm;
#pragma unroll
        for (int i = 0; i < 16; i++) { negm[i] = -mrow; o[i] = 0.f; }
        unsigned ka = sbase + L_K + lane * 16;
        unsigned va = r < 16 ? sbase + L_V + (h * 16 + r) * 16 : sbase + L_ONE + ((r == 16 || r == 28) ? 0 : ((r >= 20 && r < 28) ? 64 : 16));
        const unsigned vstep = r < 16 ? 8192u : 0u;
        asm volatile(R"(
.macro ATILE cur, nxt, tl
    v_exp_f32 v[\cur+0], v[\cur+0]
    s_waitcnt lgkmcnt(2)
    v_exp_f32 v[\cur+1], v[\cur+1]
    v_smfmac_f32_32x32x32_bf16 v[0:15], v[72:75], v[\nxt+8:\nxt+15], v83
    ds_read_b128 v[72:75], v81 offset:\tl*1024+512
    v_exp_f32 v[\cur+2], v[\cur+2]
    v_exp_f32 v[\cur+3], v[\cur+3]
    v_exp_f32 v[\cur+4], v[\cur+4]
    v_exp_f32 v[\cur+5], v[\cur+5]
    s_waitcnt lgkmcnt(2)
    v_exp_f32 v[\cur+6], v[\cur+6]
    v_mfma_f32_32x32x16_bf16 v[\nxt:\nxt+15], v[64:67], v[76:79], v[16:31]
    ds_read_b128 v[64:67], v80 offset:(\tl+2)*1024
    v_exp_f32 v[\cur+7], v[\cur+7]
    v_exp_f32 v[\cur+8], v[\cur+8]
    .if \tl == 7
    v_add_u32 v81, v82, v81
    .endif
    v_exp_f32 v[\cur+9], v[\cur+9]
    v_exp_f32 v[\cur+10], v[\cur+10]
    s_waitcnt lgkmcnt(2)
    v_exp_f32 v[\cur+11], v[\cur+11]
    v_smfmac_f32_32x32x32_bf16 v[0:15], v[68:71], v[\cur:\cur+7], v83
    ds_read_b128 v[68:71], v81 offset:((\tl+1)&7)*1024
    v_exp_f32 v[\cur+12], v[\cur+12]
    v_exp_f32 v[\cur+13], v[\cur+13]
    v_exp_f32 v[\cur+14], v[\cur+14]
    v_exp_f32 v[\cur+15], v[\cur+15]
.endm
    ds_read_b128 v[64:67], v80
    v_mov_b32 v83, 0xdddddddd
    v_mov_b32 v56, 0
    v_mov_b32 v57, 0
    v_mov_b32 v58, 0
    v_mov_b32 v59, 0
    v_mov_b32 v60, 0
    v_mov_b32 v61, 0
    v_mov_b32 v62, 0
    v_mov_b32 v63, 0
    s_mov_b32 s20, 8
    s_setprio 3
    s_waitcnt lgkmcnt(0)
    v_mfma_f32_32x32x16_bf16 v[32:47], v[64:67], v[76:79], v[16:31]
    ds_read_b128 v[72:75], v81
    ds_read_b128 v[64:67], v80 offset:1024
    ds_read_b128 v[68:71], v81
    s_nop 7
    s_nop 3
1:
    ATILE 32, 48, 0
    ATILE 48, 32, 1
    ATILE 32, 48, 2
    ATILE 48, 32, 3
    ATILE 32, 48, 4
    ATILE 48, 32, 5
    ATILE 32, 48, 6
    ATILE 48, 32, 7
    v_add_u32 v80, 0x2000, v80
    s_sub_u32 s20, s20, 1
    s_cmp_eq_u32 s20, 4
    s_cbranch_scc0 2f
    s_setprio 2
2:
    s_cmp_eq_u32 s20, 2
    s_cbranch_scc0 3f
    s_setprio 1
3:
    s_cmp_eq_u32 s20, 1
    s_cbranch_scc0 4f
    s_setprio 0
4:
    s_cmp_lg_u32 s20, 0
    s_cbranch_scc1 1b
    s_waitcnt lgkmcnt(0)
    v_smfmac_f32_32x32x32_bf16 v[0:15], v[72:75], v[56:63], v83
    s_nop 7
    s_nop 4
.purgem ATILE
)"
            : "+{v[0:15]}"(o), "+{v80}"(ka), "+{v81}"(va)
            : "{v[16:31]}"(negm), "{v[76:79]}"(qf), "{v82}"(vstep)
            : "memory", "s20", "scc",
              "v32","v33","v34","v35","v36","v37","v38","v39","v40","v41","v42","v43","v44","v45","v46","v47",
              "v48","v49","v50","v51","v52","v53","v54","v55","v56","v57","v58","v59","v60","v61","v62","v63",
              "v64","v65","v66","v67","v68","v69","v70","v71","v72","v73","v74","v75","v83");
    }
    float l = h ? o[12] : o[8];
    if (__any(!(l >= 0x1p-60f))) {
        WAITV(0);
        f32x16 z;
#pragma unroll
        for (int i = 0; i < 16; i++) z[i] = 0.f;
        float mx = -INFINITY;
        for (int t = 0; t < 64; t++) {
            f32x16 s = mfma_v(*(const bf16x8*)(kg + t * 1024 + lane * 16), qf, z);
#pragma unroll
            for (int i = 0; i < 16; i++) mx = fmaxf(mx, s[i]);
        }
        mx = fmaxf(mx, swap_half(mx));
#pragma unroll
        for (int i = 0; i < 16; i++) { z[i] = -mx; o[i] = 0.f; }
        for (int t = 0; t < 64; t++) {
            f32x16 s = mfma_v(*(const bf16x8*)(kg + t * 1024 + lane * 16), qf, z);
#pragma unroll
            for (int s2 = 0; s2 < 2; s2++) {
                u32x4 vv = *(const u32x4*)(vg + t * 1024 + s2 * 512 + (h * 16 + (r & 15)) * 16);
                if (r >= 16) vv = (u32x4){0x3f803f80u, 0x3f803f80u, 0x3f803f80u, 0x3f803f80u};
                f32x8 p;
#pragma unroll
                for (int i = 0; i < 8; i++) p[i] = __builtin_amdgcn_exp2f(s[s2 * 8 + i]);
                smfmac_v(o, vv, p, 0xddddddddu);
            }
        }
        l = o[8];
    }
    STAMPW(1, 2, 0); STAMPW(1, 3, 7);
    const rsrc_t wsr = mkrsrc(ws, 32u << 20);
    {
        const float inv = gq * __builtin_amdgcn_rcpf(l);
        u32x4 ov;
#pragma unroll
        for (int i = 0; i < 4; i++) ov[i] = pk(o[2 * i] * inv, o[2 * i + 1] * inv);
        st16(wsr, (unsigned)WS_O + ((qt * 32 + head) * 64 + lane) * 16, ov);
        WAITV(0);
        if (lane == 0) st4(wsr, (unsigned)WS_FLAG + ((qt >> 1) * 64 + (qt & 1) * 32 + head) * 4, 1u);
    }
    barrier();
    STAMPW(1, 4, 0);
#pragma unroll
    for (int n = 3; n < 12; n++) piece(n);
    f32x16 c0, c1;
#pragma unroll
    for (int i = 0; i < 16; i++) { c0[i] = 0.f; c1[i] = 0.f; }
    const int q = (lane & 15) >> 2, p = lane & 3, blk = (lane >> 4) & 1;
    const unsigned wr0 = sbase + (unsigned)((4 * h + q) * 128 + blk * 32 + p * 8) + (unsigned)(((q >> 1) & 1) * 64);
    const unsigned wr1 = wr0 ^ 64u;
    auto slot_of = [&](int rd) { return (unsigned)piece_slot((4 * rd + kq) % 20) * 8192u; };
#define ROUND(RD, A0) do { if (RD < 7) WAITV(8); else if (RD == 7) WAITV(0); barrier(); \
        const unsigned so = slot_of(RD); round_mma<A0>(c0, c1, wr0 + so, wr1 + so); \
        if (RD < 6) { piece(4 * RD + 12); piece(4 * RD + 13); piece(4 * RD + 14); piece(4 * RD + 15); } } while (0)
    ROUND(0, 0); ROUND(1, 16); ROUND(2, 32); ROUND(3, 48); ROUND(4, 64); ROUND(5, 80); ROUND(6, 96);
    STAMPW(1, 5, 0);
    if (w == 0) {
        const GAS unsigned* fl = (const GAS unsigned*)(ws + WS_FLAG) + rb * 64 + lane;
        for (int spin = 0; spin < (1 << 20); spin++) {
            const unsigned v = __hip_atomic_load(fl, __ATOMIC_RELAXED, __HIP_MEMORY_SCOPE_AGENT);
            if (__all(v == 1u)) break;
            __builtin_amdgcn_s_sleep(1);
        }
    }
    barrier();
    STAMPW(1, 6, 0);
    {
        const char* ob = ws + WS_O + (size_t)((rb * 2 + rt) * 32 + kq * 8) * 1024;
        const unsigned lo = lane * 16;
        lda_sc1<0>(ob, lo); lda_sc1<1>(ob, lo); lda_sc1<2>(ob, lo); lda_sc1<3>(ob, lo); ob += 4096;
        lda_sc1<4>(ob, lo); lda_sc1<5>(ob, lo); lda_sc1<6>(ob, lo); lda_sc1<7>(ob, lo);
    }
    asm volatile("s_nop 7\n\ts_nop 4" : "+v"(c0), "+v"(c1));
    {
        const float gb = 1.0f - go;
#pragma unroll
        for (int i = 0; i < 16; i++) { c0[i] *= gb; c1[i] *= gb; }
    }
    ROUND(7, 0);
    { const unsigned so = slot_of(8); round_mma<16>(c0, c1, wr0 + so, wr1 + so); }
    asm volatile("s_nop 7\n\ts_nop 4" : "+v"(c0), "+v"(c1));
    barrier();
#pragma unroll
    for (int g = 0; g < 4; g++) {
        *(LAS f32x4*)(uintptr_t)(sbase + w * 8192 + g * 1024 + lane * 16) = (f32x4){c0[4 * g], c0[4 * g + 1], c0[4 * g + 2], c0[4 * g + 3]};
        *(LAS f32x4*)(uintptr_t)(sbase + w * 8192 + (4 + g) * 1024 + lane * 16) = (f32x4){c1[4 * g], c1[4 * g + 1], c1[4 * g + 2], c1[4 * g + 3]};
    }
    barrier();
    {
        const unsigned ra = sbase + rt * 8192 + (2 * kq + h) * 1024 + r * 16;
        f32x4 lo4 = {0.f, 0.f, 0.f, 0.f}, hi4 = lo4;
#pragma unroll
        for (int k = 0; k < 4; k++) {
            lo4 += *(const LAS f32x4*)(uintptr_t)(ra + k * 16384);
            hi4 += *(const LAS f32x4*)(uintptr_t)(ra + k * 16384 + 512);
        }
        u32x4 ov = {pk(lo4[0], lo4[1]), pk(lo4[2], lo4[3]), pk(hi4[0], hi4[1]), pk(hi4[2], hi4[3])};
        *(u32x4*)(out + (size_t)(rb * 64 + rt * 32 + r) * D + cb * 64 + (2 * kq + h) * 8) = ov;
    }
    STAMPW(1, 7, 0);
}

extern "C" void kernel_launch(void* const* d_in, const int* in_sizes, int n_in, void* d_out, int out_size, void* d_ws, size_t ws_size,
                              hipStream_t stream) {
    const __bf16* x = (const __bf16*)d_in[0];
    const __bf16* wqkv = (const __bf16*)d_in[1];
    const __bf16* wo = (const __bf16*)d_in[2];
    const __bf16* w1 = (const __bf16*)d_in[4];
    const __bf16* b1 = (const __bf16*)d_in[5];
    const __bf16* w2 = (const __bf16*)d_in[6];
    const __bf16* gw = (const __bf16*)d_in[7];
    char* ws = (char*)d_ws;
    (void)hipFuncSetAttribute((const void*)k1_proj, hipFuncAttributeMaxDynamicSharedMemorySize, K1_LDS);
    (void)hipFuncSetAttribute((const void*)k2_attn, hipFuncAttributeMaxDynamicSharedMemorySize, K2_LDS);
    k1_proj<<<256, 512, K1_LDS, stream>>>(x, wqkv, w1, b1, gw, ws);
    k2_attn<<<256, 512, K2_LDS, stream>>>(wo, w2, ws, (__bf16*)d_out);
}
```

```cpp
#include <hip/hip_runtime.h>
#include <hip/hip_bf16.h>
#include <stdint.h>

typedef __bf16 bf16x8 __attribute__((ext_vector_type(8)));
typedef __bf16 bf16x2 __attribute__((ext_vector_type(2)));
typedef float f32x2 __attribute__((ext_vector_type(2)));
typedef float f32x4 __attribute__((ext_vector_type(4)));
typedef float f32x8 __attribute__((ext_vector_type(8)));
typedef float f32x16 __attribute__((ext_vector_type(16)));
typedef unsigned u32x4 __attribute__((ext_vector_type(4)));
typedef unsigned u32x2 __attribute__((ext_vector_type(2)));
typedef __amdgpu_buffer_rsrc_t rsrc_t;
#define LAS __attribute__((address_space(3)))
#define GAS __attribute__((address_space(1)))
#define DI __device__ __forceinline__

#define AG10(b) "a" #b "0","a" #b "1","a" #b "2","a" #b "3","a" #b "4","a" #b "5","a" #b "6","a" #b "7","a" #b "8","a" #b "9"
#define ACLOB "a0","a1","a2","a3","a4","a5","a6","a7","a8","a9", AG10(1), AG10(2), AG10(3), AG10(4), AG10(5), AG10(6), AG10(7), AG10(8), AG10(9), AG10(10), AG10(11), \
    "a120","a121","a122","a123","a124","a125","a126","a127"
#define MEM "memory", ACLOB

#if defined(__HIP_DEVICE_COMPILE__)
#pragma clang attribute push(__attribute__((target("no-packed-fp32-ops"))), apply_to = function)
#endif

constexpr int D = 512, F = 1792;
constexpr float LOG2E = 1.4426950408889634f;

constexpr unsigned WS_Q = 0;
constexpr unsigned WS_K = 2u << 20;
constexpr unsigned WS_V = 4u << 20;
constexpr unsigned WS_O = 6u << 20;
constexpr unsigned WS_ACT = 8u << 20;
constexpr unsigned WS_SM = 15u << 20;
constexpr unsigned SM_G = 0;
constexpr unsigned SM_FA = 8192;
constexpr unsigned SM_FB = SM_FA + 262144;
constexpr unsigned SM_FC = SM_FB + 131072;
constexpr unsigned SM_EP = SM_FC + 131072;
constexpr unsigned LDS_BYTES = 163840;
constexpr unsigned ONES = 0x1e000, VIMG = 0x20000, KV0 = 0xe000;
constexpr int APRIO = 2, HPRIO = 0, HPRIO2 = 0;

DI float u2f(unsigned u) { return __builtin_bit_cast(float, u); }
DI unsigned f2u(float f) { return __builtin_bit_cast(unsigned, f); }
DI bool all64(bool p) { return __builtin_amdgcn_ballot_w64(p) == ~0ull; }
DI bool any64(bool p) { return __builtin_amdgcn_ballot_w64(p) != 0ull; }
DI unsigned pk(float a, float b) { f32x2 f = {a, b}; return __builtin_bit_cast(unsigned, __builtin_convertvector(f, bf16x2)); }
DI float bflo(unsigned u) { return u2f(u << 16); }
DI float bfhi(unsigned u) { return u2f(u & 0xffff0000u); }
DI void glds16(const void* sbase, unsigned voff, unsigned lds_dst) {
    asm volatile("s_mov_b32 m0, %2\n\ts_nop 0\n\tglobal_load_lds_dwordx4 %0, %1" :: "v"(voff), "s"(sbase), "s"(lds_dst) : MEM);
}
DI const char* uni(const void* p) {
    unsigned long long v = (unsigned long long)p;
    unsigned lo = __builtin_amdgcn_readfirstlane((unsigned)v), hi = __builtin_amdgcn_readfirstlane((unsigned)(v >> 32));
    return (const char*)(((unsigned long long)hi << 32) | lo);
}
DI unsigned uni(unsigned v) { return __builtin_amdgcn_readfirstlane(v); }
#define WAITV(n) asm volatile("s_waitcnt vmcnt(" #n ")" ::: MEM)
DI void barrier() { asm volatile("s_waitcnt lgkmcnt(0)\n\ts_barrier" ::: MEM); }
DI float swap_half(float v) {
    u32x2 r = __builtin_amdgcn_permlane32_swap(f2u(v), f2u(v), false, false);
    return u2f(((__builtin_amdgcn_mbcnt_lo(-1, 0) & 32) ? r[0] : r[1]));
}
template <int CTRL> DI float dppf(float v) { return u2f(__builtin_amdgcn_update_dpp(0, f2u(v), CTRL, 0xf, 0xf, true)); }
DI float row_sum16(float v) { v += dppf<0xB1>(v); v += dppf<0x4E>(v); v += dppf<0x141>(v); v += dppf<0x140>(v); return v; }
DI float row_max16(float v) { v = __builtin_fmaxf(v, dppf<0xB1>(v)); v = __builtin_fmaxf(v, dppf<0x4E>(v)); v = __builtin_fmaxf(v, dppf<0x141>(v)); v = __builtin_fmaxf(v, dppf<0x140>(v)); return v; }
DI float rdlane(float v, int l) { return u2f(__builtin_amdgcn_readlane(f2u(v), l)); }
DI float wave_sum(float v) { v = row_sum16(v); return (rdlane(v, 0) + rdlane(v, 16)) + (rdlane(v, 32) + rdlane(v, 48)); }
DI float wave_max(float v) { v = row_max16(v); return __builtin_fmaxf(__builtin_fmaxf(rdlane(v, 0), rdlane(v, 16)), __builtin_fmaxf(rdlane(v, 32), rdlane(v, 48))); }
DI rsrc_t mkrsrc(const void* p, unsigned bytes) { return __builtin_amdgcn_make_buffer_rsrc((void*)p, 0, bytes, 0x00020000); }
DI void st16(rsrc_t rs, unsigned off, u32x4 v) { __builtin_amdgcn_raw_buffer_store_b128(v, rs, off, 0, 16); }
DI void st4(rsrc_t rs, unsigned off, unsigned v) { __builtin_amdgcn_raw_buffer_store_b32(v, rs, off, 0, 16); }
DI u32x4 ld16(rsrc_t rs, unsigned off) { return __builtin_amdgcn_raw_buffer_load_b128(rs, off, 0, 16); }

DI float mish(float hv) {
    float e = __builtin_amdgcn_exp2f(__builtin_fmaf(hv, LOG2E, -0.5f));
    float v = e + 0.70710678118654752f;
    float r = __builtin_amdgcn_rcpf(__builtin_fmaf(v, v, 0.5f));
    return __builtin_fmaf(-hv, r, hv);
}
DI f32x16 mfma_v(u32x4 a, u32x4 b, f32x16 c) {
    f32x16 d;
    asm volatile("s_nop 1\n\tv_mfma_f32_32x32x16_bf16 %0, %1, %2, %3\n\ts_nop 7\n\ts_nop 4" : "=&v"(d) : "v"(a), "v"(b), "v"(c));
    return d;
}
DI void smfmac_v(f32x16& o, u32x4 a, f32x8 p, unsigned idx) {
    asm volatile("s_nop 1\n\tv_smfmac_f32_32x32x32_bf16 %0, %1, %2, %3\n\ts_nop 7\n\ts_nop 4" : "+v"(o) : "v"(a), "v"(p), "v"(idx));
}
template <int N> DI void lda_sc1(const void* base, unsigned voff) {
    asm volatile("global_load_dwordx4 a[%c2:%c2+3], %0, %1 offset:%c3 sc1" :: "v"(voff), "s"(base), "i"(4 * N), "i"((N & 1) * 1024) : MEM);
}
template <int N> DI void sta_lds(unsigned addr) {
    asm volatile("ds_write_b128 %0, a[%c1:%c1+3] offset:%c2" :: "v"(addr), "i"(4 * N), "i"((N & 1) * 1024 + ((N >> 1) & 1) * 8192 + ((N >> 2) & 1) * 16384) : MEM);
}
DI void post(unsigned addr, unsigned v) { asm volatile("s_mov_b64 exec, 1\n\tds_write_b32 %0, %1\n\ts_mov_b64 exec, -1" :: "v"(addr), "v"(v) : MEM); }
DI void wait_ge(unsigned addr, unsigned need) {
    unsigned v, n;
    asm volatile("s_mov_b32 %1, 0x100000\n"
                 "LW1_%=:\n\tds_read_b32 %0, %2\n\ts_waitcnt lgkmcnt(0)\n\tv_cmp_le_u32 vcc, %3, %0\n\ts_nop 1\n\ts_cmp_eq_u64 vcc, -1\n\ts_cbranch_scc1 LW2_%=\n\t"
                 "s_sleep 1\n\ts_sub_u32 %1, %1, 1\n\ts_cbranch_scc0 LW1_%=\n"
                 "LW2_%=:" : "=&v"(v), "=&s"(n) : "v"(addr), "s"(need) : MEM, "scc", "vcc");
}
DI void peek(unsigned& v, unsigned addr) { asm volatile("ds_read_b32 %0, %1" : "=v"(v) : "v"(addr) : MEM); }
DI void confirm(unsigned& v, unsigned addr, unsigned need) {
    asm volatile("s_waitcnt lgkmcnt(0)" : "+v"(v) :: MEM);
    if (!all64(v >= need)) wait_ge(addr, need);
}
DI unsigned img_lds(int rt, int j) { return ((rt ? 0x00e0d0cu : 0x07131206u) >> (8 * j) & 0xffu) << 13; }
DI unsigned slot_lds(int j, int i) { return ((i == 2 ? 0x04030201u : (i ? 0x0b0a0908u : 0x07131206u)) >> (8 * j) & 0xffu) << 13; }
template <int P, int NAP = 8> DI void wait_ge_s(unsigned addr, unsigned need) {
    unsigned v, n;
    asm volatile("s_mov_b32 %1, 0x100000\n"
                 "LW1_%=:\n\tds_read_b32 %0, %2\n\ts_waitcnt lgkmcnt(0)\n\tv_cmp_le_u32 vcc, %3, %0\n\ts_nop 1\n\ts_cmp_eq_u64 vcc, -1\n\ts_cbranch_scc1 LW2_%=\n\t"
                 "s_setprio 0\n\ts_sleep %c5\n\ts_sub_u32 %1, %1, 1\n\ts_cbranch_scc0 LW1_%=\n"
                 "LW2_%=:\n\ts_setprio %c4" : "=&v"(v), "=&s"(n) : "v"(addr), "s"(need), "i"(P), "i"(NAP) : MEM, "scc", "vcc");
}
DI void poll_tag(const char* base, unsigned off, unsigned tag) {
    unsigned v, n;
    asm volatile("s_mov_b32 %1, 0x10000\n"
                 "LW1_%=:\n\tglobal_load_dword %0, %2, %4 sc1\n\ts_waitcnt vmcnt(0)\n\tv_cmp_eq_u32 vcc, %3, %0\n\ts_nop 1\n\ts_cmp_eq_u64 vcc, -1\n\ts_cbranch_scc1 LW2_%=\n\t"
                 "s_sleep 16\n\ts_sub_u32 %1, %1, 1\n\ts_cbranch_scc0 LW1_%=\n"
                 "LW2_%=:" : "=&v"(v), "=&s"(n) : "v"(off), "s"(tag), "s"(base) : MEM, "scc", "vcc");
}
template <int N, int OFF> DI void lda4(const void* base, unsigned voff) {
    asm volatile("global_load_dwordx4 a[%c2:%c2+3], %0, %1 offset:%c3 sc1" :: "v"(voff), "s"(base), "i"(4 * N), "i"(OFF) : MEM);
}
DI void g2_read(f32x16& w0, f32x16& w1, unsigned a0) {
    asm volatile(
        "ds_read_b64_tr_b16 v[64:65], v96 offset:0\n"
        "ds_read_b64_tr_b16 v[66:67], v96 offset:1024\n"
        "ds_read_b64_tr_b16 v[68:69], v97 offset:0\n"
        "ds_read_b64_tr_b16 v[70:71], v97 offset:1024\n"
        "ds_read_b64_tr_b16 v[72:73], v96 offset:2048\n"
        "ds_read_b64_tr_b16 v[74:75], v96 offset:3072\n"
        "ds_read_b64_tr_b16 v[76:77], v97 offset:2048\n"
        "ds_read_b64_tr_b16 v[78:79], v97 offset:3072\n"
        "ds_read_b64_tr_b16 v[80:81], v96 offset:4096\n"
        "ds_read_b64_tr_b16 v[82:83], v96 offset:5120\n"
        "ds_read_b64_tr_b16 v[84:85], v97 offset:4096\n"
        "ds_read_b64_tr_b16 v[86:87], v97 offset:5120\n"
        "ds_read_b64_tr_b16 v[88:89], v96 offset:6144\n"
        "ds_read_b64_tr_b16 v[90:91], v96 offset:7168\n"
        "s_waitcnt lgkmcnt(12)\n"
        "ds_read_b64_tr_b16 v[92:93], v97 offset:6144\n"
        "ds_read_b64_tr_b16 v[94:95], v97 offset:7168\n"
        "s_waitcnt lgkmcnt(0)\n"
        : "={v[64:79]}"(w0), "={v[80:95]}"(w1) : "{v96}"(a0), "{v97}"(a0 ^ 64u) : MEM);
}
template <int B, int P> DI void g2_first(f32x16& c0, f32x16& c1, const f32x16& w0, const f32x16& w1) {
    asm volatile(
        "s_setprio 0\n"
        "v_mfma_f32_32x32x16_bf16 v[0:15], v[64:67], a[%c4+0:%c4+3], 0\n"
        "v_mfma_f32_32x32x16_bf16 a[96:111], v[64:67], a[%c4+4:%c4+7], 0\n"
        "v_mfma_f32_32x32x16_bf16 v[16:31], v[68:71], a[%c4+0:%c4+3], 0\n"
        "v_mfma_f32_32x32x16_bf16 a[112:127], v[68:71], a[%c4+4:%c4+7], 0\n"
        "v_mfma_f32_32x32x16_bf16 v[0:15], v[72:75], a[%c4+8:%c4+11], v[0:15]\n"
        "v_mfma_f32_32x32x16_bf16 a[96:111], v[72:75], a[%c4+12:%c4+15], a[96:111]\n"
        "v_mfma_f32_32x32x16_bf16 v[16:31], v[76:79], a[%c4+8:%c4+11], v[16:31]\n"
        "v_mfma_f32_32x32x16_bf16 a[112:127], v[76:79], a[%c4+12:%c4+15], a[112:127]\n"
        "v_mfma_f32_32x32x16_bf16 v[0:15], v[80:83], a[%c4+16:%c4+19], v[0:15]\n"
        "v_mfma_f32_32x32x16_bf16 a[96:111], v[80:83], a[%c4+20:%c4+23], a[96:111]\n"
        "v_mfma_f32_32x32x16_bf16 v[16:31], v[84:87], a[%c4+16:%c4+19], v[16:31]\n"
        "v_mfma_f32_32x32x16_bf16 a[112:127], v[84:87], a[%c4+20:%c4+23], a[112:127]\n"
        "v_mfma_f32_32x32x16_bf16 v[0:15], v[88:91], a[%c4+24:%c4+27], v[0:15]\n"
        "v_mfma_f32_32x32x16_bf16 a[96:111], v[88:91], a[%c4+28:%c4+31], a[96:111]\n"
        "v_mfma_f32_32x32x16_bf16 v[16:31], v[92:95], a[%c4+24:%c4+27], v[16:31]\n"
        "v_mfma_f32_32x32x16_bf16 a[112:127], v[92:95], a[%c4+28:%c4+31], a[112:127]\n"
        "s_setprio %c5\n"
        : "={v[0:15]}"(c0), "={v[16:31]}"(c1) : "{v[64:79]}"(w0), "{v[80:95]}"(w1), "i"(B), "i"(P) : MEM);
}
template <int B, int P> DI void g2_mma(f32x16& c0, f32x16& c1, const f32x16& w0, const f32x16& w1) {
    asm volatile(
        "s_setprio 0\n"
        "v_mfma_f32_32x32x16_bf16 v[0:15], v[64:67], a[%c4+0:%c4+3], v[0:15]\n"
        "v_mfma_f32_32x32x16_bf16 a[96:111], v[64:67], a[%c4+4:%c4+7], a[96:111]\n"
        "v_mfma_f32_32x32x16_bf16 v[16:31], v[68:71], a[%c4+0:%c4+3], v[16:31]\n"
        "v_mfma_f32_32x32x16_bf16 a[112:127], v[68:71], a[%c4+4:%c4+7], a[112:127]\n"
        "v_mfma_f32_32x32x16_bf16 v[0:15], v[72:75], a[%c4+8:%c4+11], v[0:15]\n"
        "v_mfma_f32_32x32x16_bf16 a[96:111], v[72:75], a[%c4+12:%c4+15], a[96:111]\n"
        "v_mfma_f32_32x32x16_bf16 v[16:31], v[76:79], a[%c4+8:%c4+11], v[16:31]\n"
        "v_mfma_f32_32x32x16_bf16 a[112:127], v[76:79], a[%c4+12:%c4+15], a[112:127]\n"
        "v_mfma_f32_32x32x16_bf16 v[0:15], v[80:83], a[%c4+16:%c4+19], v[0:15]\n"
        "v_mfma_f32_32x32x16_bf16 a[96:111], v[80:83], a[%c4+20:%c4+23], a[96:111]\n"
        "v_mfma_f32_32x32x16_bf16 v[16:31], v[84:87], a[%c4+16:%c4+19], v[16:31]\n"
        "v_mfma_f32_32x32x16_bf16 a[112:127], v[84:87], a[%c4+20:%c4+23], a[112:127]\n"
        "v_mfma_f32_32x32x16_bf16 v[0:15], v[88:91], a[%c4+24:%c4+27], v[0:15]\n"
        "v_mfma_f32_32x32x16_bf16 a[96:111], v[88:91], a[%c4+28:%c4+31], a[96:111]\n"
        "v_mfma_f32_32x32x16_bf16 v[16:31], v[92:95], a[%c4+24:%c4+27], v[16:31]\n"
        "v_mfma_f32_32x32x16_bf16 a[112:127], v[92:95], a[%c4+28:%c4+31], a[112:127]\n"
        "s_setprio %c5\n"
        : "+{v[0:15]}"(c0), "+{v[16:31]}"(c1) : "{v[64:79]}"(w0), "{v[80:95]}"(w1), "i"(B), "i"(P) : MEM);
}

__global__ __launch_bounds__(512, 2) void fused(const __bf16* __restrict__ x, const __bf16* __restrict__ wqkv, const __bf16* __restrict__ wo,
                                                 const __bf16* __restrict__ w1, const __bf16* __restrict__ b1, const __bf16* __restrict__ w2,
                                                 const __bf16* __restrict__ gate_w, char* __restrict__ ws, __bf16* __restrict__ out) {
    extern __shared__ __attribute__((aligned(16))) char smem[];
    const unsigned sbase = (unsigned)(uintptr_t)(LAS char*)smem;
    const int w = __builtin_amdgcn_readfirstlane(__builtin_amdgcn_workitem_id_x() >> 6);
    const int bid = __builtin_amdgcn_workgroup_id_x();
    asm volatile("" ::: ACLOB);
    const int xg = bid & 7, li = bid >> 3;
    const int rb1 = (xg >> 2) * 8 + (li & 7);
    const int j4 = li >> 3, c16 = (xg & 3) * 4 + j4;
    const int ui = c16 >> 1, par = c16 & 1;
    constexpr int rot = 0;
    const int ua = (xg & 3) * 7 + 2 * j4, ub = j4 == 3 ? ua - 1 : ua + 1;
    const int head = xg * 4 + (li & 3), qb = li >> 2;
    const int rb = head, cb = qb;
    char* const sm = ws + WS_SM;
    const rsrc_t wsr = mkrsrc(ws, 32u << 20);
    const int lw = w & 3;
    const int rtile = rb1 * 4 + lw;
    unsigned tag;
    const unsigned pw = sbase + ONES + 0x1f00;
    const unsigned po = pw + lw * 4;
#define LANE_(l) asm volatile("v_mbcnt_lo_u32_b32 %0, -1, 0\n\tv_mbcnt_hi_u32_b32 %0, -1, %0" : "=v"(l) :: ACLOB)
#define LANEVALS int lane; LANE_(lane); \
    const int r = lane & 31, h = lane >> 5, lr = lane >> 3, lp = lane & 7, tq = (lane & 15) >> 2, tp = lane & 3, tb = (lane >> 4) & 1, xf = (r >> 1) & 7; \
    const unsigned lo16 = lane * 16, pl = pw + (lane & 3) * 4, la = sbase + lw * 2048 + lo16, xrow = sbase + (unsigned)((lw * 32 + r) * 128); \
    const unsigned wsw16 = (unsigned)((lp ^ (((lr >> 1) & 1) << 2)) << 4), trc = sbase + (unsigned)(tq * 128 + tb * 32 + tp * 8) + (unsigned)(((tq >> 1) & 1) * 64); \
    const unsigned wrd = trc + h * 1024, wr0 = trc + h * 512; \
    (void)xf; (void)la; (void)xrow; (void)wsw16; (void)wrd; (void)wr0; (void)pl
#define CODE_WARM { unsigned long long pc_; int len_; \
        asm volatile("s_getpc_b64 %0\nLcode_first:\n\ts_mov_b32 %1, Lcode_last-Lcode_first\n\ts_max_i32 %1, %1, 0" : "=s"(pc_), "=s"(len_) :: "scc"); \
        asm volatile("global_load_dword a126, %0, %1" :: "v"(__builtin_elementwise_min(((li * 4 + w) * 4 + (lane & 3)) * 64, len_)), "s"(pc_) : MEM); }
    unsigned bias[4], ep0;
    u32x4 gw4, xa4, xb4;
    const int tok = rtile * 32 + 2 * c16;
    if (w >= 4) {
        LANEVALS;
        const unsigned xoff = (unsigned)((rb1 * 128 + lw * 32 + lr) * 1024 + ((lp ^ (lr >> 1)) << 4));
        const unsigned qoff = (unsigned)((lw * 16 + lr) * 3072 + par * 1024 + ui * 128) + wsw16;
        auto stage_op = [&](int s, int k) {
            const unsigned dst = sbase + (unsigned)(s % 5) * 0x6000u;
            const int kc = (s + rot) & 7;
            if (k < 4) glds16((const char*)x + kc * 128 + k * 8192, (k & 1) ? xoff ^ 64u : xoff, dst + lw * 4096 + k * 1024);
            else glds16((const char*)wqkv + kc * (64 * 3072) + (k - 4) * (8 * 3072), qoff, dst + 16384 + lw * 2048 + (k - 4) * 1024);
        };
        __builtin_amdgcn_s_setprio(3);
#pragma unroll
        for (int i = 0; i < 12; i++) stage_op(i / 6, i % 6);
    } else {
        LANEVALS;
        __builtin_amdgcn_s_setprio(3);
        CODE_WARM
        asm volatile("global_load_dwordx4 %0, %2, %3\n\tglobal_load_dwordx4 %1, %2, %3 offset:1024" : "=&v"(xa4), "=&v"(xb4) : "v"(lo16 + tok * 1024u), "s"(x) : MEM);
        asm volatile("global_load_dwordx4 %0, %1, %2" : "=v"(gw4) : "v"(lo16), "s"(gate_w) : MEM);
        asm volatile("global_load_dword %0, %1, %2 sc1" : "=v"(ep0) : "v"(SM_EP), "s"(sm) : MEM);
        asm volatile("global_load_ushort %0, %4, %6\n\tglobal_load_ushort %1, %4, %6 offset:64\n\tglobal_load_ushort %2, %5, %6\n\tglobal_load_ushort %3, %5, %6 offset:64"
                     : "=&v"(bias[0]), "=&v"(bias[1]), "=&v"(bias[2]), "=&v"(bias[3]) : "v"((unsigned)(ua * 128 + r * 2)), "v"((unsigned)(ub * 128 + r * 2)), "s"(b1) : MEM);
    }
    asm volatile("" :: "s"(wo), "s"(w1), "s"(b1), "s"(w2), "s"(gate_w), "s"(out));
    const char* kg = ws + WS_K + head * 65536 + lw * 2048;
    f32x16 b0, b1v;
    float gv;
    u32x4 of0, of1, of2, of3, of4, of5, of6, of7;
#define O_LOADS asm volatile( \
        "global_load_dwordx4 v[32:35], v100, s[40:41] offset:0 sc1\n" \
        "global_load_dwordx4 v[36:39], v100, s[40:41] offset:1024 sc1\n" \
        "global_load_dwordx4 v[40:43], v100, s[40:41] offset:2048 sc1\n" \
        "global_load_dwordx4 v[44:47], v100, s[40:41] offset:3072 sc1\n" \
        "global_load_dwordx4 v[48:51], v101, s[40:41] offset:0 sc1\n" \
        "global_load_dwordx4 v[52:55], v101, s[40:41] offset:1024 sc1\n" \
        "global_load_dwordx4 v[56:59], v101, s[40:41] offset:2048 sc1\n" \
        "global_load_dwordx4 v[60:63], v101, s[40:41] offset:3072 sc1\n" \
        : "={v[32:35]}"(of0), "={v[36:39]}"(of1), "={v[40:43]}"(of2), "={v[44:47]}"(of3), "={v[48:51]}"(of4), "={v[52:55]}"(of5), "={v[56:59]}"(of6), "={v[60:63]}"(of7) \
        : "{v100}"(lo16_), "{v101}"(lo16_ + 4096u), "{s[40:41]}"(ob) : MEM)
#define LDC(c) { const char* kc_ = kg + c * 8192; lda_sc1<4 * c>(kc_, lo16); lda_sc1<4 * c + 1>(kc_, lo16); \
                 lda_sc1<4 * c + 2>(kc_ + (WS_V - WS_K), lo16); lda_sc1<4 * c + 3>(kc_ + (WS_V - WS_K), lo16); }
#define STC(c, R) sta_lds<4 * c>(la + R); sta_lds<4 * c + 1>(la + R); sta_lds<4 * c + 2>(la + R); sta_lds<4 * c + 3>(la + R);
    const char* ob = uni(ws + WS_O + ((rb * 2 + (w >> 2)) * 32 + lw * 8) * 1024);

    if (w >= 4) {
        LANEVALS;
        const unsigned foff = (unsigned)((lw * 16 + lr) * (F * 2)) + wsw16;
        auto w1_op = [&](int u, int c, int k) {
            const int kc = (c + rot) & 7;
            const unsigned dst = c < 6 ? (u ? 0u : 0x12000u) + c * 8192u : (u ? VIMG : KV0) + (c - 6) * 8192u;
            glds16((const char*)w1 + kc * (64 * F * 2) + k * (8 * F * 2) + (u ? ub : ua) * 128, foff, sbase + dst + lw * 2048 + k * 1024);
        };
        const unsigned xoff = (unsigned)((rb1 * 128 + lw * 32 + lr) * 1024 + ((lp ^ (lr >> 1)) << 4));
        const unsigned qoff = (unsigned)((lw * 16 + lr) * 3072 + par * 1024 + ui * 128) + wsw16;
        const unsigned voff = (unsigned)((lw * 16 + (lane >> 2)) * 3072 + 2048 + ui * 128 + par * 64 + (lane & 3) * 16);
        auto stage_op = [&](int s, int k) {
            const unsigned dst = sbase + (unsigned)(s % 5) * 0x6000u;
            const int kc = (s + rot) & 7;
            if (k < 4) glds16((const char*)x + kc * 128 + k * 8192, (k & 1) ? xoff ^ 64u : xoff, dst + lw * 4096 + k * 1024);
            else glds16((const char*)wqkv + kc * (64 * 3072) + (k - 4) * (8 * 3072), qoff, dst + 16384 + lw * 2048 + (k - 4) * 1024);
        };
        auto v_op = [&](int c) { glds16((const char*)wqkv + ((c + rot) & 7) * (64 * 3072), voff, sbase + VIMG + c * 4096 + lw * 1024); };
        unsigned ep;
        unsigned xa[4];
#pragma unroll
        for (int ks = 0; ks < 4; ks++) xa[ks] = xrow + (unsigned)(((2 * ks + h) ^ xf) << 4);
        const unsigned fw = po, cw = pl + 16;
        unsigned cv;
        auto v_pass = [&]() {
            f32x16 accv;
            asm volatile(
        "s_waitcnt lgkmcnt(0)\n"
        "ds_read_b64_tr_b16 v[64:65], v100 offset:0\n"
        "ds_read_b64_tr_b16 v[66:67], v100 offset:256\n"
        "ds_read_b64_tr_b16 v[68:69], v100 offset:1024\n"
        "ds_read_b64_tr_b16 v[70:71], v100 offset:1280\n"
        "ds_read_b64_tr_b16 v[72:73], v100 offset:2048\n"
        "ds_read_b64_tr_b16 v[74:75], v100 offset:2304\n"
        "ds_read_b64_tr_b16 v[76:77], v100 offset:3072\n"
        "ds_read_b64_tr_b16 v[78:79], v100 offset:3328\n"
        "ds_read_b64_tr_b16 v[80:81], v100 offset:4096\n"
        "ds_read_b64_tr_b16 v[82:83], v100 offset:4352\n"
        "ds_read_b64_tr_b16 v[84:85], v100 offset:5120\n"
        "ds_read_b64_tr_b16 v[86:87], v100 offset:5376\n"
        "ds_read_b64_tr_b16 v[88:89], v100 offset:6144\n"
        "ds_read_b64_tr_b16 v[90:91], v100 offset:6400\n"
        "s_waitcnt lgkmcnt(12)\n"
        "v_mfma_f32_32x32x16_bf16 v[32:47], a[0:3], v[64:67], 0\n"
        "ds_read_b64_tr_b16 v[92:93], v100 offset:7168\n"
        "ds_read_b64_tr_b16 v[94:95], v100 offset:7424\n"
        "s_waitcnt lgkmcnt(12)\n"
        "v_mfma_f32_32x32x16_bf16 v[32:47], a[4:7], v[68:71], v[32:47]\n"
        "ds_read_b64_tr_b16 v[64:65], v100 offset:8192\n"
        "ds_read_b64_tr_b16 v[66:67], v100 offset:8448\n"
        "s_waitcnt lgkmcnt(12)\n"
        "v_mfma_f32_32x32x16_bf16 v[32:47], a[8:11], v[72:75], v[32:47]\n"
        "ds_read_b64_tr_b16 v[68:69], v100 offset:9216\n"
        "ds_read_b64_tr_b16 v[70:71], v100 offset:9472\n"
        "s_waitcnt lgkmcnt(12)\n"
        "v_mfma_f32_32x32x16_bf16 v[32:47], a[12:15], v[76:79], v[32:47]\n"
        "ds_read_b64_tr_b16 v[72:73], v100 offset:10240\n"
        "ds_read_b64_tr_b16 v[74:75], v100 offset:10496\n"
        "s_waitcnt lgkmcnt(12)\n"
        "v_mfma_f32_32x32x16_bf16 v[32:47], a[16:19], v[80:83], v[32:47]\n"
        "ds_read_b64_tr_b16 v[76:77], v100 offset:11264\n"
        "ds_read_b64_tr_b16 v[78:79], v100 offset:11520\n"
        "s_waitcnt lgkmcnt(12)\n"
        "v_mfma_f32_32x32x16_bf16 v[32:47], a[20:23], v[84:87], v[32:47]\n"
        "ds_read_b64_tr_b16 v[80:81], v100 offset:12288\n"
        "ds_read_b64_tr_b16 v[82:83], v100 offset:12544\n"
        "s_waitcnt lgkmcnt(12)\n"
        "v_mfma_f32_32x32x16_bf16 v[32:47], a[24:27], v[88:91], v[32:47]\n"
        "ds_read_b64_tr_b16 v[84:85], v100 offset:13312\n"
        "ds_read_b64_tr_b16 v[86:87], v100 offset:13568\n"
        "s_waitcnt lgkmcnt(12)\n"
        "v_mfma_f32_32x32x16_bf16 v[32:47], a[28:31], v[92:95], v[32:47]\n"
        "ds_read_b64_tr_b16 v[88:89], v100 offset:14336\n"
        "ds_read_b64_tr_b16 v[90:91], v100 offset:14592\n"
        "s_waitcnt lgkmcnt(12)\n"
        "v_mfma_f32_32x32x16_bf16 v[32:47], a[32:35], v[64:67], v[32:47]\n"
        "ds_read_b64_tr_b16 v[92:93], v100 offset:15360\n"
        "ds_read_b64_tr_b16 v[94:95], v100 offset:15616\n"
        "s_waitcnt lgkmcnt(12)\n"
        "v_mfma_f32_32x32x16_bf16 v[32:47], a[36:39], v[68:71], v[32:47]\n"
        "ds_read_b64_tr_b16 v[64:65], v100 offset:16384\n"
        "ds_read_b64_tr_b16 v[66:67], v100 offset:16640\n"
        "s_waitcnt lgkmcnt(12)\n"
        "v_mfma_f32_32x32x16_bf16 v[32:47], a[40:43], v[72:75], v[32:47]\n"
        "ds_read_b64_tr_b16 v[68:69], v100 offset:17408\n"
        "ds_read_b64_tr_b16 v[70:71], v100 offset:17664\n"
        "s_waitcnt vmcnt(0)\n"
        "v_mov_b32 v98, 12\n"
        "s_mov_b64 exec, 1\n"
        "ds_write_b32 v99, v98\n"
        "s_mov_b64 exec, -1\n"
        "s_waitcnt lgkmcnt(12)\n"
        "v_mfma_f32_32x32x16_bf16 v[32:47], a[44:47], v[76:79], v[32:47]\n"
        "ds_read_b64_tr_b16 v[72:73], v100 offset:18432\n"
        "ds_read_b64_tr_b16 v[74:75], v100 offset:18688\n"
        "s_waitcnt lgkmcnt(12)\n"
        "v_mfma_f32_32x32x16_bf16 v[32:47], a[48:51], v[80:83], v[32:47]\n"
        "ds_read_b64_tr_b16 v[76:77], v100 offset:19456\n"
        "ds_read_b64_tr_b16 v[78:79], v100 offset:19712\n"
        "s_waitcnt lgkmcnt(12)\n"
        "v_mfma_f32_32x32x16_bf16 v[32:47], a[52:55], v[84:87], v[32:47]\n"
        "ds_read_b64_tr_b16 v[80:81], v100 offset:20480\n"
        "ds_read_b64_tr_b16 v[82:83], v100 offset:20736\n"
        "s_waitcnt lgkmcnt(12)\n"
        "v_mfma_f32_32x32x16_bf16 v[32:47], a[56:59], v[88:91], v[32:47]\n"
        "ds_read_b64_tr_b16 v[84:85], v100 offset:21504\n"
        "ds_read_b64_tr_b16 v[86:87], v100 offset:21760\n"
        "s_waitcnt lgkmcnt(12)\n"
        "v_mfma_f32_32x32x16_bf16 v[32:47], a[60:63], v[92:95], v[32:47]\n"
        "ds_read_b64_tr_b16 v[88:89], v100 offset:22528\n"
        "ds_read_b64_tr_b16 v[90:91], v100 offset:22784\n"
        "s_waitcnt lgkmcnt(12)\n"
        "v_mfma_f32_32x32x16_bf16 v[32:47], a[64:67], v[64:67], v[32:47]\n"
        "ds_read_b64_tr_b16 v[92:93], v100 offset:23552\n"
        "ds_read_b64_tr_b16 v[94:95], v100 offset:23808\n"
        "s_waitcnt lgkmcnt(12)\n"
        "v_mfma_f32_32x32x16_bf16 v[32:47], a[68:71], v[68:71], v[32:47]\n"
        "ds_read_b64_tr_b16 v[64:65], v100 offset:24576\n"
        "ds_read_b64_tr_b16 v[66:67], v100 offset:24832\n"
        "s_waitcnt lgkmcnt(12)\n"
        "v_mfma_f32_32x32x16_bf16 v[32:47], a[72:75], v[72:75], v[32:47]\n"
        "ds_read_b64_tr_b16 v[68:69], v100 offset:25600\n"
        "ds_read_b64_tr_b16 v[70:71], v100 offset:25856\n"
        "s_waitcnt lgkmcnt(12)\n"
        "v_mfma_f32_32x32x16_bf16 v[32:47], a[76:79], v[76:79], v[32:47]\n"
        "ds_read_b64_tr_b16 v[72:73], v100 offset:26624\n"
        "ds_read_b64_tr_b16 v[74:75], v100 offset:26880\n"
        "s_waitcnt lgkmcnt(12)\n"
        "v_mfma_f32_32x32x16_bf16 v[32:47], a[80:83], v[80:83], v[32:47]\n"
        "ds_read_b64_tr_b16 v[76:77], v100 offset:27648\n"
        "ds_read_b64_tr_b16 v[78:79], v100 offset:27904\n"
        "s_waitcnt lgkmcnt(12)\n"
        "v_mfma_f32_32x32x16_bf16 v[32:47], a[84:87], v[84:87], v[32:47]\n"
        "ds_read_b64_tr_b16 v[80:81], v100 offset:28672\n"
        "ds_read_b64_tr_b16 v[82:83], v100 offset:28928\n"
        "s_waitcnt lgkmcnt(12)\n"
        "v_mfma_f32_32x32x16_bf16 v[32:47], a[88:91], v[88:91], v[32:47]\n"
        "ds_read_b64_tr_b16 v[84:85], v100 offset:29696\n"
        "ds_read_b64_tr_b16 v[86:87], v100 offset:29952\n"
        "s_waitcnt lgkmcnt(12)\n"
        "v_mfma_f32_32x32x16_bf16 v[32:47], a[92:95], v[92:95], v[32:47]\n"
        "ds_read_b64_tr_b16 v[88:89], v100 offset:30720\n"
        "ds_read_b64_tr_b16 v[90:91], v100 offset:30976\n"
        "s_waitcnt lgkmcnt(12)\n"
        "v_mfma_f32_32x32x16_bf16 v[32:47], a[96:99], v[64:67], v[32:47]\n"
        "ds_read_b64_tr_b16 v[92:93], v100 offset:31744\n"
        "ds_read_b64_tr_b16 v[94:95], v100 offset:32000\n"
        "s_waitcnt lgkmcnt(12)\n"
        "v_mfma_f32_32x32x16_bf16 v[32:47], a[100:103], v[68:71], v[32:47]\n"
        "s_waitcnt lgkmcnt(10)\n"
        "v_mfma_f32_32x32x16_bf16 v[32:47], a[104:107], v[72:75], v[32:47]\n"
        "s_waitcnt lgkmcnt(8)\n"
        "v_mfma_f32_32x32x16_bf16 v[32:47], a[108:111], v[76:79], v[32:47]\n"
        "s_waitcnt lgkmcnt(6)\n"
        "v_mfma_f32_32x32x16_bf16 v[32:47], a[112:115], v[80:83], v[32:47]\n"
        "s_waitcnt lgkmcnt(4)\n"
        "v_mfma_f32_32x32x16_bf16 v[32:47], a[116:119], v[84:87], v[32:47]\n"
        "s_waitcnt lgkmcnt(2)\n"
        "v_mfma_f32_32x32x16_bf16 v[32:47], a[120:123], v[88:91], v[32:47]\n"
        "s_waitcnt lgkmcnt(0)\n"
        "v_mfma_f32_32x32x16_bf16 v[32:47], a[124:127], v[92:95], v[32:47]\n"
        "s_nop 7\n"
        "s_nop 4\n"
                : "={v[32:47]}"(accv) : "{v100}"(sbase + VIMG + (unsigned)((8 * h + tq) * 64 + tb * 32 + tp * 8)), "{v99}"(fw) : MEM, "v64","v65","v66","v67","v68","v69","v70","v71","v72","v73","v74","v75","v76","v77","v78","v79","v80","v81","v82","v83","v84","v85","v86","v87","v88","v89","v90","v91","v92","v93","v94","v95", "v98");
#pragma unroll
            for (int hh = 0; hh < 2; hh++) {
                u32x4 o;
#pragma unroll
                for (int i = 0; i < 4; i++) o[i] = pk(accv[hh * 8 + 2 * i], accv[hh * 8 + 2 * i + 1]);
                st16(wsr, WS_V + ((ui * 4 + par * 2 + (r >> 4)) * 64 + rtile) * 1024 + hh * 512 + (h * 16 + (r & 15)) * 16, o);
            }
        };
#define V_PASS __builtin_amdgcn_s_setprio(3); v_pass();
#define V_FLAG { asm volatile("" : "+v"(ep)); tag = __builtin_amdgcn_readfirstlane(ep) + 1u; \
            if (lane == 0) __builtin_amdgcn_raw_buffer_store_b64((u32x2){0u, tag}, wsr, WS_SM + SM_FA + (unsigned)(((16 + c16) * 64 + rtile) * 128), 0, 16); }
#define EARLY_LOADS \
        asm volatile("global_load_dword %0, %1, %2 sc1" : "=v"(ep) : "v"(SM_EP), "s"(sm) : MEM); \
        barrier();
#define XRD(s, k) asm volatile("ds_read_b128 a[%c2:%c2+3], %0 offset:%c3\n\tds_read_b128 a[%c2+4:%c2+7], %1 offset:%c3" \
            :: "v"(xa[2 * k] + ((s % 5) / 3) * 0x12000u), "v"(xa[2 * k + 1] + ((s % 5) / 3) * 0x12000u), "i"(16 * s + 8 * k), "i"(((s % 5) % 3) * 0x6000) : MEM)
        EARLY_LOADS
        stage_op(2, 0);
        WAITV(8); post(fw, 1);
        stage_op(2, 1);
        XRD(0, 0);
        stage_op(2, 2);
        XRD(0, 1);
        stage_op(2, 3);
        stage_op(2, 4);
        stage_op(2, 5);
        stage_op(3, 0);
        WAITV(8); post(fw, 2);
        stage_op(3, 1);
        XRD(1, 0);
        stage_op(3, 2);
        XRD(1, 1);
        stage_op(3, 3);
        stage_op(3, 4);
        stage_op(3, 5);
        stage_op(4, 0);
        WAITV(7); post(fw, 3);
        stage_op(4, 1);
        XRD(2, 0);
        stage_op(4, 2);
        XRD(2, 1);
        stage_op(4, 3);
        peek(cv, cw);
        stage_op(4, 4);
        stage_op(4, 5);
        confirm(cv, cw, 1);
        stage_op(5, 0);
        WAITV(7); post(fw, 4);
        stage_op(5, 1);
        XRD(3, 0);
        stage_op(5, 2);
        XRD(3, 1);
        stage_op(5, 3);
        peek(cv, cw);
        stage_op(5, 4);
        stage_op(5, 5);
        confirm(cv, cw, 2);
        stage_op(6, 0);
        WAITV(7); post(fw, 5);
        stage_op(6, 1);
        XRD(4, 0);
        stage_op(6, 2);
        XRD(4, 1);
        stage_op(6, 3);
        peek(cv, cw);
        stage_op(6, 4);
        stage_op(6, 5);
        confirm(cv, cw, 3);
        stage_op(7, 0);
        WAITV(7); post(fw, 6);
        stage_op(7, 1);
        XRD(5, 0);
        stage_op(7, 2);
        XRD(5, 1);
        stage_op(7, 3);
        stage_op(7, 4);
        stage_op(7, 5);
        WAITV(6); post(fw, 7);
        v_op(0);
        XRD(6, 0);
        v_op(1);
        XRD(6, 1);
        v_op(2);
        WAITV(3); post(fw, 8); __builtin_amdgcn_s_setprio(1);
        v_op(3);
        XRD(7, 0);
        v_op(4);
        XRD(7, 1);
        v_op(5);
        peek(cv, cw);
        v_op(6);
        v_op(7);
        confirm(cv, cw, 4);
        w1_op(0, 0, 0);
        w1_op(0, 0, 1);
        w1_op(0, 1, 0);
        w1_op(0, 1, 1);
        peek(cv, cw);
        w1_op(0, 2, 0);
        w1_op(0, 2, 1);
        WAITV(6); post(fw, 9);
        confirm(cv, cw, 5);
        w1_op(0, 3, 0);
        w1_op(0, 3, 1);
        w1_op(0, 4, 0);
        w1_op(0, 4, 1);
        peek(cv, cw);
        w1_op(0, 5, 0);
        w1_op(0, 5, 1);
        WAITV(6); post(fw, 10);
        wait_ge(pl, 9);
        confirm(cv, cw, 8);
        w1_op(0, 6, 0);
        w1_op(0, 6, 1);
        w1_op(0, 7, 0);
        w1_op(0, 7, 1);
        WAITV(4); post(fw, 11);
        V_PASS post(fw, 13);
        w1_op(1, 0, 0);
        w1_op(1, 0, 1);
        w1_op(1, 1, 0);
        w1_op(1, 1, 1);
        w1_op(1, 2, 0);
        w1_op(1, 2, 1);
        WAITV(6); V_FLAG
        w1_op(1, 3, 0);
        w1_op(1, 3, 1);
        w1_op(1, 4, 0);
        w1_op(1, 4, 1);
        w1_op(1, 5, 0);
        w1_op(1, 5, 1);
        wait_ge(pl, 13);
        w1_op(1, 6, 0);
        w1_op(1, 6, 1);
        w1_op(1, 7, 0);
        w1_op(1, 7, 1);
        WAITV(4); post(fw, 14); WAITV(0); post(fw, 15);
        if (w == 4) {
            u32x2 fa, fb, fc;
            const unsigned faoff = SM_FA + (unsigned)(((head >> 2) * 128 + qb * 8 + (lane & 7)) * 128);
            const unsigned fboff = SM_FA + (unsigned)(((head >> 2) * 128 + 64 + lane) * 128), fcoff = SM_FA + (unsigned)(((16 + (head >> 1)) * 64 + lane) * 128);
            for (int spin = 0; spin < (1 << 16); spin++) {
                asm volatile("global_load_dwordx2 %0, %3, %4 sc1\n\tglobal_load_dwordx2 %1, %5, %4 sc1\n\tglobal_load_dwordx2 %2, %6, %4 sc1\n\ts_waitcnt vmcnt(0)"
                             : "=&v"(fa), "=&v"(fb), "=&v"(fc) : "v"(faoff), "s"(sm), "v"(fboff), "v"(fcoff) : MEM);
                if (all64(fa[1] == tag && fb[1] == tag && fc[1] == tag)) break;
            }
            post(pw + 64, 1);
            post(pw + 68, f2u(wave_max(u2f(fb[0]))));
        } else {
            wait_ge(pw + 64, 1);
        }
        u32x4 q0, q1;
        const int qt = qb * 8 + 2 * lw;
        asm volatile("global_load_dwordx4 %0, %2, %3 sc1\n\tglobal_load_dwordx4 %1, %2, %3 offset:1024 sc1" : "=&v"(q0), "=&v"(q1) : "v"(lo16), "s"(ws + WS_Q + (head * 64 + qt) * 1024) : MEM);
        LDC(0) LDC(1)
        wait_ge(pl + 32, 1);
        WAITV(4); STC(0, KV0) WAITV(0); STC(1, KV0)
        post(po + 48, 1);
        asm volatile("" : "+v"(q0), "+v"(q1));
        float n0 = 0.f, n1 = 0.f;
#pragma unroll
        for (int i = 0; i < 4; i++) {
            n0 += bflo(q0[i]) * bflo(q0[i]) + bfhi(q0[i]) * bfhi(q0[i]);
            n1 += bflo(q1[i]) * bflo(q1[i]) + bfhi(q1[i]) * bfhi(q1[i]);
        }
        n0 += swap_half(n0); n1 += swap_half(n1);
        __builtin_amdgcn_s_setprio(APRIO);
        wait_ge(pl + 48, 1);
        const float km = *(const LAS float*)(uintptr_t)(pw + 68);
        const float mrow = __builtin_amdgcn_sqrtf(__builtin_fmaxf(n0, n1) * km);
        f32x16 o0, o1;
        {
            f32x16 negm;
#pragma unroll
            for (int i = 0; i < 16; i++) { negm[i] = 48.0f - mrow; o0[i] = 0.f; o1[i] = 0.f; }
            unsigned ka = sbase + KV0 + lo16;
            unsigned va = r < 16 ? sbase + KV0 + 8192 + (h * 16 + r) * 16 : sbase + ONES + ((0x0ff00000u >> r) & 1u) * 64u + ((0xe00e0000u >> r) & 1u) * 16u;
            asm volatile(
        "s_mov_b32 s59, 0x100000\n"
        "s_mov_b32 s54, 0xf3020202\n"
        "s_mov_b32 s55, 0x000c0202\n"
        "s_mov_b32 s52, 0\n"
        "v_mov_b32 v84, 0\n"
        "ds_read_b128 v[96:99], v124\n"
        "ds_read_b128 v[100:103], v125\n"
        "ds_read_b128 v[104:107], v125 offset:512\n"
        "v_mov_b32 v126, 0xdddddddd\n"
        "v_mov_b32 v72, 0\n"
        "v_mov_b32 v73, 0\n"
        "v_mov_b32 v74, 0\n"
        "v_mov_b32 v75, 0\n"
        "v_mov_b32 v76, 0\n"
        "v_mov_b32 v77, 0\n"
        "v_mov_b32 v78, 0\n"
        "v_mov_b32 v79, 0\n"
        "v_mov_b32 v116, 0\n"
        "v_mov_b32 v117, 0\n"
        "v_mov_b32 v118, 0\n"
        "v_mov_b32 v119, 0\n"
        "s_waitcnt lgkmcnt(0)\n"
        "v_mfma_f32_32x32x16_bf16 v[48:63], v[96:99], v[108:111], v[32:47]\n"
        "s_nop 7\n"
        "s_nop 4\n"
        "LA%=:\n"
        "v_exp_f32 v48, v48\n"
        "v_exp_f32 v49, v49\n"
        "v_smfmac_f32_32x32x32_bf16 v[16:31], v[116:119], v[72:79], v126\n"
        "ds_read_b128 v[116:119], v125 offset:1536\n"
        "v_exp_f32 v50, v50\n"
        "v_exp_f32 v51, v51\n"
        "v_exp_f32 v52, v52\n"
        "v_mfma_f32_32x32x16_bf16 v[64:79], v[96:99], v[120:123], v[32:47]\n"
        "ds_read_b128 v[96:99], v124 offset:1024\n"
        "v_exp_f32 v53, v53\n"
        "v_exp_f32 v54, v54\n"
        "v_exp_f32 v55, v55\n"
        "v_exp_f32 v56, v56\n"
        "v_exp_f32 v57, v57\n"
        "v_smfmac_f32_32x32x32_bf16 v[0:15], v[100:103], v[48:55], v126\n"
        "ds_read_b128 v[112:115], v125 offset:1024\n"
        "v_exp_f32 v58, v58\n"
        "v_exp_f32 v59, v59\n"
        "v_exp_f32 v60, v60\n"
        "v_exp_f32 v61, v61\n"
        "v_exp_f32 v62, v62\n"
        "v_exp_f32 v63, v63\n"
        "v_exp_f32 v64, v64\n"
        "v_exp_f32 v65, v65\n"
        "v_smfmac_f32_32x32x32_bf16 v[0:15], v[104:107], v[56:63], v126\n"
        "v_exp_f32 v66, v66\n"
        "v_exp_f32 v67, v67\n"
        "s_waitcnt lgkmcnt(0)\n"
        "v_exp_f32 v68, v68\n"
        "v_mfma_f32_32x32x16_bf16 v[48:63], v[96:99], v[108:111], v[32:47]\n"
        "v_exp_f32 v69, v69\n"
        "v_exp_f32 v70, v70\n"
        "v_exp_f32 v71, v71\n"
        "v_exp_f32 v72, v72\n"
        "v_exp_f32 v73, v73\n"
        "v_smfmac_f32_32x32x32_bf16 v[16:31], v[100:103], v[64:71], v126\n"
        "v_exp_f32 v74, v74\n"
        "v_exp_f32 v75, v75\n"
        "v_exp_f32 v76, v76\n"
        "v_exp_f32 v77, v77\n"
        "v_exp_f32 v78, v78\n"
        "v_exp_f32 v79, v79\n"
        "v_exp_f32 v48, v48\n"
        "v_exp_f32 v49, v49\n"
        "v_smfmac_f32_32x32x32_bf16 v[16:31], v[104:107], v[72:79], v126\n"
        "ds_read_b128 v[104:107], v125 offset:2560\n"
        "v_exp_f32 v50, v50\n"
        "v_exp_f32 v51, v51\n"
        "v_exp_f32 v52, v52\n"
        "v_mfma_f32_32x32x16_bf16 v[64:79], v[96:99], v[120:123], v[32:47]\n"
        "ds_read_b128 v[96:99], v124 offset:2048\n"
        "v_exp_f32 v53, v53\n"
        "v_exp_f32 v54, v54\n"
        "v_exp_f32 v55, v55\n"
        "v_exp_f32 v56, v56\n"
        "v_exp_f32 v57, v57\n"
        "v_smfmac_f32_32x32x32_bf16 v[0:15], v[112:115], v[48:55], v126\n"
        "ds_read_b128 v[100:103], v125 offset:2048\n"
        "v_exp_f32 v58, v58\n"
        "v_exp_f32 v59, v59\n"
        "v_exp_f32 v60, v60\n"
        "v_exp_f32 v61, v61\n"
        "v_exp_f32 v62, v62\n"
        "v_exp_f32 v63, v63\n"
        "v_exp_f32 v64, v64\n"
        "v_exp_f32 v65, v65\n"
        "v_smfmac_f32_32x32x32_bf16 v[0:15], v[116:119], v[56:63], v126\n"
        "v_exp_f32 v66, v66\n"
        "v_exp_f32 v67, v67\n"
        "s_waitcnt lgkmcnt(0)\n"
        "v_exp_f32 v68, v68\n"
        "v_mfma_f32_32x32x16_bf16 v[48:63], v[96:99], v[108:111], v[32:47]\n"
        "v_exp_f32 v69, v69\n"
        "v_exp_f32 v70, v70\n"
        "v_exp_f32 v71, v71\n"
        "v_exp_f32 v72, v72\n"
        "v_exp_f32 v73, v73\n"
        "v_smfmac_f32_32x32x32_bf16 v[16:31], v[112:115], v[64:71], v126\n"
        "v_exp_f32 v74, v74\n"
        "v_exp_f32 v75, v75\n"
        "v_exp_f32 v76, v76\n"
        "v_exp_f32 v77, v77\n"
        "v_exp_f32 v78, v78\n"
        "v_exp_f32 v79, v79\n"
        "v_exp_f32 v48, v48\n"
        "v_exp_f32 v49, v49\n"
        "v_smfmac_f32_32x32x32_bf16 v[16:31], v[116:119], v[72:79], v126\n"
        "ds_read_b128 v[116:119], v125 offset:3584\n"
        "v_exp_f32 v50, v50\n"
        "v_exp_f32 v51, v51\n"
        "v_exp_f32 v52, v52\n"
        "v_mfma_f32_32x32x16_bf16 v[64:79], v[96:99], v[120:123], v[32:47]\n"
        "ds_read_b128 v[96:99], v124 offset:3072\n"
        "v_exp_f32 v53, v53\n"
        "v_exp_f32 v54, v54\n"
        "v_exp_f32 v55, v55\n"
        "v_exp_f32 v56, v56\n"
        "v_exp_f32 v57, v57\n"
        "v_smfmac_f32_32x32x32_bf16 v[0:15], v[100:103], v[48:55], v126\n"
        "ds_read_b128 v[112:115], v125 offset:3072\n"
        "v_exp_f32 v58, v58\n"
        "v_exp_f32 v59, v59\n"
        "v_exp_f32 v60, v60\n"
        "v_exp_f32 v61, v61\n"
        "v_exp_f32 v62, v62\n"
        "v_exp_f32 v63, v63\n"
        "v_exp_f32 v64, v64\n"
        "v_exp_f32 v65, v65\n"
        "v_smfmac_f32_32x32x32_bf16 v[0:15], v[104:107], v[56:63], v126\n"
        "v_exp_f32 v66, v66\n"
        "v_exp_f32 v67, v67\n"
        "s_waitcnt lgkmcnt(0)\n"
        "v_exp_f32 v68, v68\n"
        "v_mfma_f32_32x32x16_bf16 v[48:63], v[96:99], v[108:111], v[32:47]\n"
        "s_sext_i32_i8 s53, s54\n"
        "s_lshr_b64 s[54:55], s[54:55], 8\n"
        "v_exp_f32 v69, v69\n"
        "v_exp_f32 v70, v70\n"
        "v_exp_f32 v71, v71\n"
        "v_exp_f32 v72, v72\n"
        "v_exp_f32 v73, v73\n"
        "v_smfmac_f32_32x32x32_bf16 v[16:31], v[100:103], v[64:71], v126\n"
        "v_exp_f32 v74, v74\n"
        "v_exp_f32 v75, v75\n"
        "v_exp_f32 v76, v76\n"
        "v_exp_f32 v77, v77\n"
        "v_exp_f32 v78, v78\n"
        "v_exp_f32 v79, v79\n"
        "v_exp_f32 v48, v48\n"
        "v_exp_f32 v49, v49\n"
        "v_smfmac_f32_32x32x32_bf16 v[16:31], v[104:107], v[72:79], v126\n"
        "ds_read_b128 v[104:107], v125 offset:4608\n"
        "v_exp_f32 v50, v50\n"
        "v_exp_f32 v51, v51\n"
        "v_exp_f32 v52, v52\n"
        "v_mfma_f32_32x32x16_bf16 v[64:79], v[96:99], v[120:123], v[32:47]\n"
        "ds_read_b128 v[96:99], v124 offset:4096\n"
        "v_exp_f32 v53, v53\n"
        "v_exp_f32 v54, v54\n"
        "v_exp_f32 v55, v55\n"
        "v_exp_f32 v56, v56\n"
        "v_exp_f32 v57, v57\n"
        "v_smfmac_f32_32x32x32_bf16 v[0:15], v[112:115], v[48:55], v126\n"
        "ds_read_b128 v[100:103], v125 offset:4096\n"
        "v_exp_f32 v58, v58\n"
        "v_exp_f32 v59, v59\n"
        "v_exp_f32 v60, v60\n"
        "v_exp_f32 v61, v61\n"
        "v_exp_f32 v62, v62\n"
        "v_exp_f32 v63, v63\n"
        "v_exp_f32 v64, v64\n"
        "v_exp_f32 v65, v65\n"
        "v_smfmac_f32_32x32x32_bf16 v[0:15], v[116:119], v[56:63], v126\n"
        "v_exp_f32 v66, v66\n"
        "v_exp_f32 v67, v67\n"
        "s_waitcnt lgkmcnt(0)\n"
        "v_exp_f32 v68, v68\n"
        "v_mfma_f32_32x32x16_bf16 v[48:63], v[96:99], v[108:111], v[32:47]\n"
        "s_lshl_b32 s53, s53, 13\n"
        "v_add_u32 v84, 1, v84\n"
        "v_exp_f32 v69, v69\n"
        "v_exp_f32 v70, v70\n"
        "v_exp_f32 v71, v71\n"
        "v_exp_f32 v72, v72\n"
        "v_exp_f32 v73, v73\n"
        "v_smfmac_f32_32x32x32_bf16 v[16:31], v[112:115], v[64:71], v126\n"
        "v_exp_f32 v74, v74\n"
        "v_exp_f32 v75, v75\n"
        "v_exp_f32 v76, v76\n"
        "v_exp_f32 v77, v77\n"
        "v_exp_f32 v78, v78\n"
        "v_exp_f32 v79, v79\n"
        "v_exp_f32 v48, v48\n"
        "v_exp_f32 v49, v49\n"
        "v_smfmac_f32_32x32x32_bf16 v[16:31], v[116:119], v[72:79], v126\n"
        "ds_read_b128 v[116:119], v125 offset:5632\n"
        "v_exp_f32 v50, v50\n"
        "v_exp_f32 v51, v51\n"
        "v_exp_f32 v52, v52\n"
        "v_mfma_f32_32x32x16_bf16 v[64:79], v[96:99], v[120:123], v[32:47]\n"
        "ds_read_b128 v[96:99], v124 offset:5120\n"
        "v_exp_f32 v53, v53\n"
        "v_exp_f32 v54, v54\n"
        "v_exp_f32 v55, v55\n"
        "v_exp_f32 v56, v56\n"
        "v_exp_f32 v57, v57\n"
        "v_smfmac_f32_32x32x32_bf16 v[0:15], v[100:103], v[48:55], v126\n"
        "ds_read_b128 v[112:115], v125 offset:5120\n"
        "v_exp_f32 v58, v58\n"
        "v_exp_f32 v59, v59\n"
        "v_exp_f32 v60, v60\n"
        "v_exp_f32 v61, v61\n"
        "v_exp_f32 v62, v62\n"
        "v_exp_f32 v63, v63\n"
        "v_exp_f32 v64, v64\n"
        "v_exp_f32 v65, v65\n"
        "v_smfmac_f32_32x32x32_bf16 v[0:15], v[104:107], v[56:63], v126\n"
        "v_exp_f32 v66, v66\n"
        "v_exp_f32 v67, v67\n"
        "s_waitcnt lgkmcnt(0)\n"
        "v_exp_f32 v68, v68\n"
        "v_mfma_f32_32x32x16_bf16 v[48:63], v[96:99], v[108:111], v[32:47]\n"
        "v_and_b32 v85, s53, v80\n"
        "v_exp_f32 v69, v69\n"
        "v_exp_f32 v70, v70\n"
        "v_exp_f32 v71, v71\n"
        "v_exp_f32 v72, v72\n"
        "v_exp_f32 v73, v73\n"
        "v_smfmac_f32_32x32x32_bf16 v[16:31], v[100:103], v[64:71], v126\n"
        "v_exp_f32 v74, v74\n"
        "v_exp_f32 v75, v75\n"
        "v_exp_f32 v76, v76\n"
        "v_exp_f32 v77, v77\n"
        "v_exp_f32 v78, v78\n"
        "v_exp_f32 v79, v79\n"
        "v_exp_f32 v48, v48\n"
        "v_exp_f32 v49, v49\n"
        "v_smfmac_f32_32x32x32_bf16 v[16:31], v[104:107], v[72:79], v126\n"
        "ds_read_b128 v[104:107], v125 offset:6656\n"
        "v_exp_f32 v50, v50\n"
        "v_exp_f32 v51, v51\n"
        "v_exp_f32 v52, v52\n"
        "v_mfma_f32_32x32x16_bf16 v[64:79], v[96:99], v[120:123], v[32:47]\n"
        "ds_read_b128 v[96:99], v124 offset:6144\n"
        "v_exp_f32 v53, v53\n"
        "v_exp_f32 v54, v54\n"
        "v_exp_f32 v55, v55\n"
        "v_exp_f32 v56, v56\n"
        "v_exp_f32 v57, v57\n"
        "v_smfmac_f32_32x32x32_bf16 v[0:15], v[112:115], v[48:55], v126\n"
        "ds_read_b128 v[100:103], v125 offset:6144\n"
        "v_exp_f32 v58, v58\n"
        "v_exp_f32 v59, v59\n"
        "v_exp_f32 v60, v60\n"
        "v_exp_f32 v61, v61\n"
        "v_exp_f32 v62, v62\n"
        "v_exp_f32 v63, v63\n"
        "v_exp_f32 v64, v64\n"
        "v_exp_f32 v65, v65\n"
        "v_smfmac_f32_32x32x32_bf16 v[0:15], v[116:119], v[56:63], v126\n"
        "v_exp_f32 v66, v66\n"
        "v_exp_f32 v67, v67\n"
        "s_waitcnt lgkmcnt(0)\n"
        "v_exp_f32 v68, v68\n"
        "v_mfma_f32_32x32x16_bf16 v[48:63], v[96:99], v[108:111], v[32:47]\n"
        "ds_read_b32 v83, v82\n"
        "v_exp_f32 v69, v69\n"
        "v_exp_f32 v70, v70\n"
        "v_exp_f32 v71, v71\n"
        "v_exp_f32 v72, v72\n"
        "v_exp_f32 v73, v73\n"
        "v_smfmac_f32_32x32x32_bf16 v[16:31], v[112:115], v[64:71], v126\n"
        "v_exp_f32 v74, v74\n"
        "v_exp_f32 v75, v75\n"
        "v_exp_f32 v76, v76\n"
        "v_exp_f32 v77, v77\n"
        "v_exp_f32 v78, v78\n"
        "v_exp_f32 v79, v79\n"
        "v_exp_f32 v48, v48\n"
        "v_exp_f32 v49, v49\n"
        "v_smfmac_f32_32x32x32_bf16 v[16:31], v[116:119], v[72:79], v126\n"
        "ds_read_b128 v[116:119], v125 offset:7680\n"
        "v_exp_f32 v50, v50\n"
        "v_exp_f32 v51, v51\n"
        "v_exp_f32 v52, v52\n"
        "v_mfma_f32_32x32x16_bf16 v[64:79], v[96:99], v[120:123], v[32:47]\n"
        "ds_read_b128 v[96:99], v124 offset:7168\n"
        "v_exp_f32 v53, v53\n"
        "v_exp_f32 v54, v54\n"
        "v_exp_f32 v55, v55\n"
        "v_exp_f32 v56, v56\n"
        "v_exp_f32 v57, v57\n"
        "v_smfmac_f32_32x32x32_bf16 v[0:15], v[100:103], v[48:55], v126\n"
        "ds_read_b128 v[112:115], v125 offset:7168\n"
        "v_exp_f32 v58, v58\n"
        "v_exp_f32 v59, v59\n"
        "v_exp_f32 v60, v60\n"
        "v_exp_f32 v61, v61\n"
        "v_exp_f32 v62, v62\n"
        "v_exp_f32 v63, v63\n"
        "v_exp_f32 v64, v64\n"
        "v_exp_f32 v65, v65\n"
        "v_smfmac_f32_32x32x32_bf16 v[0:15], v[104:107], v[56:63], v126\n"
        "v_exp_f32 v66, v66\n"
        "v_exp_f32 v67, v67\n"
        "s_waitcnt lgkmcnt(0)\n"
        "v_exp_f32 v68, v68\n"
        "v_mfma_f32_32x32x16_bf16 v[48:63], v[96:99], v[108:111], v[32:47]\n"
        "v_cmp_gt_u32 vcc, s52, v83\n"
        "v_add_u32 v124, s53, v124\n"
        "v_exp_f32 v69, v69\n"
        "v_exp_f32 v70, v70\n"
        "v_exp_f32 v71, v71\n"
        "v_exp_f32 v72, v72\n"
        "v_exp_f32 v73, v73\n"
        "v_smfmac_f32_32x32x32_bf16 v[16:31], v[100:103], v[64:71], v126\n"
        "v_exp_f32 v74, v74\n"
        "v_exp_f32 v75, v75\n"
        "v_exp_f32 v76, v76\n"
        "v_exp_f32 v77, v77\n"
        "v_exp_f32 v78, v78\n"
        "v_exp_f32 v79, v79\n"
        "v_exp_f32 v48, v48\n"
        "v_exp_f32 v49, v49\n"
        "v_smfmac_f32_32x32x32_bf16 v[16:31], v[104:107], v[72:79], v126\n"
        "s_cbranch_vccnz LKs%=\n"
        "LKr%=:\n"
        "v_add_u32 v125, v85, v125\n"
        "ds_read_b128 v[104:107], v125 offset:512\n"
        "v_exp_f32 v50, v50\n"
        "v_exp_f32 v51, v51\n"
        "v_exp_f32 v52, v52\n"
        "v_mfma_f32_32x32x16_bf16 v[64:79], v[96:99], v[120:123], v[32:47]\n"
        "ds_read_b128 v[96:99], v124 offset:0\n"
        "v_exp_f32 v53, v53\n"
        "v_exp_f32 v54, v54\n"
        "v_exp_f32 v55, v55\n"
        "v_exp_f32 v56, v56\n"
        "v_exp_f32 v57, v57\n"
        "v_smfmac_f32_32x32x32_bf16 v[0:15], v[112:115], v[48:55], v126\n"
        "ds_read_b128 v[100:103], v125 offset:0\n"
        "v_exp_f32 v58, v58\n"
        "v_exp_f32 v59, v59\n"
        "v_exp_f32 v60, v60\n"
        "v_exp_f32 v61, v61\n"
        "v_exp_f32 v62, v62\n"
        "v_exp_f32 v63, v63\n"
        "v_exp_f32 v64, v64\n"
        "v_exp_f32 v65, v65\n"
        "v_smfmac_f32_32x32x32_bf16 v[0:15], v[116:119], v[56:63], v126\n"
        "ds_write_b32 v81, v84\n"
        "v_exp_f32 v66, v66\n"
        "v_exp_f32 v67, v67\n"
        "s_waitcnt lgkmcnt(1)\n"
        "v_exp_f32 v68, v68\n"
        "v_mfma_f32_32x32x16_bf16 v[48:63], v[96:99], v[108:111], v[32:47]\n"
        "s_max_u32 s52, s52, 2\n"
        "s_add_u32 s52, s52, 1\n"
        "s_cmp_lt_u32 s52, 10\n"
        "v_exp_f32 v69, v69\n"
        "v_exp_f32 v70, v70\n"
        "v_exp_f32 v71, v71\n"
        "v_exp_f32 v72, v72\n"
        "v_exp_f32 v73, v73\n"
        "v_smfmac_f32_32x32x32_bf16 v[16:31], v[112:115], v[64:71], v126\n"
        "v_exp_f32 v74, v74\n"
        "v_exp_f32 v75, v75\n"
        "v_exp_f32 v76, v76\n"
        "v_exp_f32 v77, v77\n"
        "v_exp_f32 v78, v78\n"
        "v_exp_f32 v79, v79\n"
        "s_cbranch_scc1 LA%=\n"
        "s_nop 1\n"
        "v_smfmac_f32_32x32x32_bf16 v[16:31], v[116:119], v[72:79], v126\n"
        "s_branch LKe%=\n"
        "LKs%=:\n"
        "ds_read_b32 v83, v82\n"
        "s_waitcnt lgkmcnt(0)\n"
        "LKa%=:\n"
        "v_cmp_le_u32 vcc, s52, v83\n"
        "s_nop 1\n"
        "s_cmp_eq_u64 vcc, -1\n"
        "s_cbranch_scc1 LKb%=\n"
        "ds_read_b32 v83, v82\n"
        "s_sub_u32 s59, s59, 1\n"
        "s_waitcnt lgkmcnt(0)\n"
        "s_cbranch_scc0 LKa%=\n"
        "LKb%=:\n"
        "s_branch LKr%=\n"
        "LKe%=:\n"
        "s_waitcnt lgkmcnt(0)\n"
        "s_nop 7\n"
        "s_nop 3\n"
                : "+{v[0:15]}"(o0), "+{v[16:31]}"(o1), "+{v124}"(ka), "+{v125}"(va)
                : "{v[32:47]}"(negm), "{v[108:111]}"(q0), "{v[120:123]}"(q1), "{v80}"(r < 16 ? ~0u : 0u), "{v81}"(po + 96), "{v82}"(pl + 80)
                : MEM, "scc", "vcc", "s52", "s53", "s54", "s55", "s59", "v48","v49","v50","v51","v52","v53","v54","v55","v56","v57","v58","v59","v60","v61","v62","v63","v64","v65","v66","v67","v68","v69","v70","v71","v72","v73","v74","v75","v76","v77","v78","v79", "v83", "v84", "v85", "v96","v97","v98","v99","v100","v101","v102","v103","v104","v105","v106","v107", "v112","v113","v114","v115","v116","v117","v118","v119", "v126");
        }
        int lane_; LANE_(lane_);
        const int r_ = lane_ & 31, h_ = lane_ >> 5;
        const unsigned lo16_ = lane_ * 16;
        auto finish = [&](f32x16& o, const u32x4& qf, int tile) {
            float l = h_ ? o[12] : o[8];
            float om = l;
#pragma unroll
            for (int i = 0; i < 8; i++) om = __builtin_fmaxf(om, __builtin_fabsf(o[i]));
            if (any64(!(l >= 0x1p-60f) || !(om < __builtin_inff()) || o[0] + o[1] + o[2] + o[3] + o[4] + o[5] + o[6] + o[7] != o[0] + o[1] + o[2] + o[3] + o[4] + o[5] + o[6] + o[7])) {
                const unsigned kf = WS_K + head * 65536 + lo16_, vg = WS_V + head * 65536 + (h_ * 16 + (r_ & 15)) * 16;
                f32x16 z;
#pragma unroll
                for (int i = 0; i < 16; i++) z[i] = 0.f;
                float mx = -__builtin_inff();
                for (int t = 0; t < 64; t++) {
                    f32x16 s = mfma_v(ld16(wsr, kf + t * 1024), qf, z);
#pragma unroll
                    for (int i = 0; i < 16; i++) mx = __builtin_fmaxf(mx, s[i]);
                }
                mx = __builtin_fmaxf(mx, swap_half(mx));
#pragma unroll
                for (int i = 0; i < 16; i++) { z[i] = -mx; o[i] = 0.f; }
                for (int t = 0; t < 64; t++) {
                    f32x16 s = mfma_v(ld16(wsr, kf + t * 1024), qf, z);
#pragma unroll
                    for (int s2 = 0; s2 < 2; s2++) {
                        u32x4 vv = ld16(wsr, vg + t * 1024 + s2 * 512);
                        if (r_ >= 16) vv = (u32x4){0x3f803f80u, 0x3f803f80u, 0x3f803f80u, 0x3f803f80u};
                        f32x8 p;
#pragma unroll
                        for (int i = 0; i < 8; i++) p[i] = __builtin_amdgcn_exp2f(s[s2 * 8 + i]);
                        smfmac_v(o, vv, p, 0xddddddddu);
                    }
                }
                l = o[8];
            }
            const float inv = __builtin_amdgcn_rcpf(l);
            u32x4 ov;
#pragma unroll
            for (int i = 0; i < 4; i++) ov[i] = __builtin_elementwise_min(pk(o[2 * i] * inv, o[2 * i + 1] * inv), 0xfffffffeu);
            st16(wsr, WS_O + (tile * 32 + head) * 1024 + lo16_, ov);
        };
        finish(o0, q0, qt); finish(o1, q1, qt + 1);
        __builtin_amdgcn_s_setprio(0);
        wait_ge(pw + 112 + lw * 4, 1);
        O_LOADS;
        wait_ge(pw + 112 + lw * 4, 2);
        const unsigned hb = sbase + img_lds(1, lw) + lo16_;
#pragma unroll
        for (int i = 0; i < 4; i++) {
            const f32x4 u = *(const LAS f32x4*)(uintptr_t)(hb + i * 1024), v = *(const LAS f32x4*)(uintptr_t)(hb + (4 + i) * 1024);
#pragma unroll
            for (int e = 0; e < 4; e++) { b0[4 * i + e] = u[e]; b1v[4 * i + e] = v[e]; }
        }
        gv = *(const LAS float*)(uintptr_t)(pw - 512 + r_ * 4);
    } else {
      {
        LANEVALS;
        if (w == 0) *(LAS unsigned*)(uintptr_t)(pw + lane * 4) = 0u;
        if (w == 1) {
            const unsigned one = (lane & 3) == 0 ? 0x3f803f80u : 0u;
            *(LAS u32x4*)(uintptr_t)(sbase + ONES + (lane >> 2) * 512 + ((lane & 2) ? 64 : (lane & 1) * 16)) = (u32x4){one, one, one, one};
        }
        f32x16 acc0, acc1;
        barrier();
        WAITV(0);
        asm volatile("" : "+v"(xa4), "+v"(xb4), "+v"(gw4), "+v"(ep0), "+v"(bias[0]), "+v"(bias[1]), "+v"(bias[2]), "+v"(bias[3]));
        float gate;
        {
            float g0 = 0.f, g1 = 0.f;
#pragma unroll
            for (int i = 0; i < 4; i++) {
                g0 += bflo(xa4[i]) * bflo(gw4[i]) + bfhi(xa4[i]) * bfhi(gw4[i]);
                g1 += bflo(xb4[i]) * bflo(gw4[i]) + bfhi(xb4[i]) * bfhi(gw4[i]);
            }
            g0 = wave_sum(g0); g1 = wave_sum(g1);
            gate = __builtin_amdgcn_rcpf(1.0f + __builtin_amdgcn_exp2f(-LOG2E * ((lane & 1) ? g1 : g0)));
        }
        tag = __builtin_amdgcn_readfirstlane(ep0) + 1u;
        if (w < 2) asm volatile("global_load_dword a127, %0, %1" :: "v"((unsigned)(((li & 7) * 64 + lane) * (F * 2) + (w ? ub : ua) * 128)), "s"(w1) : MEM);

        __builtin_amdgcn_s_setprio(2);
        {
            unsigned x0 = xrow + (unsigned)(((0 + h) ^ xf) << 4), x1 = xrow + (unsigned)(((2 + h) ^ xf) << 4);
            unsigned x2 = xrow + (unsigned)(((4 + h) ^ xf) << 4), x3 = xrow + (unsigned)(((6 + h) ^ xf) << 4);
            unsigned wa = wrd, wb = wrd ^ 64u;
            asm volatile(
        "s_mov_b32 s59, 0x100000\n"
        "ds_read_b32 v104, v103\n"
        "s_waitcnt lgkmcnt(0)\n"
        "LA0a%=:\n"
        "v_cmp_le_u32 vcc, 1, v104\n"
        "s_nop 1\n"
        "s_cmp_eq_u64 vcc, -1\n"
        "s_cbranch_scc1 LA0b%=\n"
        "ds_read_b32 v104, v103\n"
        "s_sub_u32 s59, s59, 1\n"
        "s_waitcnt lgkmcnt(0)\n"
        "s_cbranch_scc0 LA0a%=\n"
        "LA0b%=:\n"
        "ds_read_b128 a[0:3], v96\n"
        "ds_read_b64_tr_b16 v[64:65], v100 offset:16384\n"
        "ds_read_b64_tr_b16 v[66:67], v100 offset:16896\n"
        "ds_read_b64_tr_b16 v[68:69], v101 offset:16384\n"
        "ds_read_b64_tr_b16 v[70:71], v101 offset:16896\n"
        "ds_read_b128 a[4:7], v97\n"
        "ds_read_b64_tr_b16 v[72:73], v100 offset:18432\n"
        "ds_read_b64_tr_b16 v[74:75], v100 offset:18944\n"
        "ds_read_b32 v104, v103\n"
        "ds_read_b64_tr_b16 v[76:77], v101 offset:18432\n"
        "ds_read_b64_tr_b16 v[78:79], v101 offset:18944\n"
        "ds_read_b128 a[8:11], v98\n"
        "ds_read_b64_tr_b16 v[80:81], v100 offset:20480\n"
        "ds_read_b64_tr_b16 v[82:83], v100 offset:20992\n"
        "s_waitcnt lgkmcnt(11)\n"
        "v_mfma_f32_32x32x16_bf16 v[0:15], v[64:67], a[0:3], 0\n"
        "ds_read_b64_tr_b16 v[84:85], v101 offset:20480\n"
        "ds_read_b64_tr_b16 v[86:87], v101 offset:20992\n"
        "s_waitcnt lgkmcnt(11)\n"
        "v_mfma_f32_32x32x16_bf16 v[16:31], v[68:71], a[0:3], 0\n"
        "ds_read_b128 a[12:15], v99\n"
        "ds_read_b64_tr_b16 v[88:89], v100 offset:22528\n"
        "ds_read_b64_tr_b16 v[90:91], v100 offset:23040\n"
        "s_waitcnt lgkmcnt(11)\n"
        "v_mfma_f32_32x32x16_bf16 v[0:15], v[72:75], a[4:7], v[0:15]\n"
        "ds_read_b64_tr_b16 v[92:93], v101 offset:22528\n"
        "ds_read_b64_tr_b16 v[94:95], v101 offset:23040\n"
        "s_waitcnt lgkmcnt(10)\n"
        "v_mfma_f32_32x32x16_bf16 v[16:31], v[76:79], a[4:7], v[16:31]\n"
        "v_add_u32 v96, 0x6000, v96\n"
        "v_add_u32 v97, 0x6000, v97\n"
        "v_add_u32 v98, 0x6000, v98\n"
        "v_add_u32 v99, 0x6000, v99\n"
        "v_add_u32 v100, 0x6000, v100\n"
        "v_add_u32 v101, 0x6000, v101\n"
        "LA1a%=:\n"
        "v_cmp_le_u32 vcc, 2, v104\n"
        "s_nop 1\n"
        "s_cmp_eq_u64 vcc, -1\n"
        "s_cbranch_scc1 LA1b%=\n"
        "ds_read_b32 v104, v103\n"
        "s_sub_u32 s59, s59, 1\n"
        "s_waitcnt lgkmcnt(0)\n"
        "s_cbranch_scc0 LA1a%=\n"
        "LA1b%=:\n"
        "ds_read_b128 a[16:19], v96\n"
        "ds_read_b64_tr_b16 v[64:65], v100 offset:16384\n"
        "ds_read_b64_tr_b16 v[66:67], v100 offset:16896\n"
        "s_waitcnt lgkmcnt(10)\n"
        "v_mfma_f32_32x32x16_bf16 v[0:15], v[80:83], a[8:11], v[0:15]\n"
        "ds_read_b64_tr_b16 v[68:69], v101 offset:16384\n"
        "ds_read_b64_tr_b16 v[70:71], v101 offset:16896\n"
        "s_waitcnt lgkmcnt(10)\n"
        "v_mfma_f32_32x32x16_bf16 v[16:31], v[84:87], a[8:11], v[16:31]\n"
        "ds_read_b128 a[20:23], v97\n"
        "ds_read_b64_tr_b16 v[72:73], v100 offset:18432\n"
        "ds_read_b64_tr_b16 v[74:75], v100 offset:18944\n"
        "ds_read_b32 v104, v103\n"
        "s_waitcnt lgkmcnt(11)\n"
        "v_mfma_f32_32x32x16_bf16 v[0:15], v[88:91], a[12:15], v[0:15]\n"
        "ds_read_b64_tr_b16 v[76:77], v101 offset:18432\n"
        "ds_read_b64_tr_b16 v[78:79], v101 offset:18944\n"
        "s_waitcnt lgkmcnt(11)\n"
        "v_mfma_f32_32x32x16_bf16 v[16:31], v[92:95], a[12:15], v[16:31]\n"
        "v_mov_b32 v106, 1\n"
        "s_mov_b64 exec, 1\n"
        "ds_write_b32 v105, v106\n"
        "s_mov_b64 exec, -1\n"
        "ds_read_b128 a[24:27], v98\n"
        "ds_read_b64_tr_b16 v[80:81], v100 offset:20480\n"
        "ds_read_b64_tr_b16 v[82:83], v100 offset:20992\n"
        "s_waitcnt lgkmcnt(12)\n"
        "v_mfma_f32_32x32x16_bf16 v[0:15], v[64:67], a[16:19], v[0:15]\n"
        "ds_read_b64_tr_b16 v[84:85], v101 offset:20480\n"
        "ds_read_b64_tr_b16 v[86:87], v101 offset:20992\n"
        "s_waitcnt lgkmcnt(12)\n"
        "v_mfma_f32_32x32x16_bf16 v[16:31], v[68:71], a[16:19], v[16:31]\n"
        "ds_read_b128 a[28:31], v99\n"
        "ds_read_b64_tr_b16 v[88:89], v100 offset:22528\n"
        "ds_read_b64_tr_b16 v[90:91], v100 offset:23040\n"
        "s_waitcnt lgkmcnt(12)\n"
        "v_mfma_f32_32x32x16_bf16 v[0:15], v[72:75], a[20:23], v[0:15]\n"
        "ds_read_b64_tr_b16 v[92:93], v101 offset:22528\n"
        "ds_read_b64_tr_b16 v[94:95], v101 offset:23040\n"
        "s_waitcnt lgkmcnt(11)\n"
        "v_mfma_f32_32x32x16_bf16 v[16:31], v[76:79], a[20:23], v[16:31]\n"
        "v_add_u32 v96, 0x6000, v96\n"
        "v_add_u32 v97, 0x6000, v97\n"
        "v_add_u32 v98, 0x6000, v98\n"
        "v_add_u32 v99, 0x6000, v99\n"
        "v_add_u32 v100, 0x6000, v100\n"
        "v_add_u32 v101, 0x6000, v101\n"
        "LA2a%=:\n"
        "v_cmp_le_u32 vcc, 3, v104\n"
        "s_nop 1\n"
        "s_cmp_eq_u64 vcc, -1\n"
        "s_cbranch_scc1 LA2b%=\n"
        "ds_read_b32 v104, v103\n"
        "s_sub_u32 s59, s59, 1\n"
        "s_waitcnt lgkmcnt(0)\n"
        "s_cbranch_scc0 LA2a%=\n"
        "LA2b%=:\n"
        "ds_read_b128 a[32:35], v96\n"
        "ds_read_b64_tr_b16 v[64:65], v100 offset:16384\n"
        "ds_read_b64_tr_b16 v[66:67], v100 offset:16896\n"
        "s_waitcnt lgkmcnt(10)\n"
        "v_mfma_f32_32x32x16_bf16 v[0:15], v[80:83], a[24:27], v[0:15]\n"
        "ds_read_b64_tr_b16 v[68:69], v101 offset:16384\n"
        "ds_read_b64_tr_b16 v[70:71], v101 offset:16896\n"
        "s_waitcnt lgkmcnt(10)\n"
        "v_mfma_f32_32x32x16_bf16 v[16:31], v[84:87], a[24:27], v[16:31]\n"
        "ds_read_b128 a[36:39], v97\n"
        "ds_read_b64_tr_b16 v[72:73], v100 offset:18432\n"
        "ds_read_b64_tr_b16 v[74:75], v100 offset:18944\n"
        "ds_read_b32 v104, v103\n"
        "s_waitcnt lgkmcnt(11)\n"
        "v_mfma_f32_32x32x16_bf16 v[0:15], v[88:91], a[28:31], v[0:15]\n"
        "ds_read_b64_tr_b16 v[76:77], v101 offset:18432\n"
        "ds_read_b64_tr_b16 v[78:79], v101 offset:18944\n"
        "s_waitcnt lgkmcnt(11)\n"
        "v_mfma_f32_32x32x16_bf16 v[16:31], v[92:95], a[28:31], v[16:31]\n"
        "v_mov_b32 v106, 2\n"
        "s_mov_b64 exec, 1\n"
        "ds_write_b32 v105, v106\n"
        "s_mov_b64 exec, -1\n"
        "ds_read_b128 a[40:43], v98\n"
        "ds_read_b64_tr_b16 v[80:81], v100 offset:20480\n"
        "ds_read_b64_tr_b16 v[82:83], v100 offset:20992\n"
        "s_waitcnt lgkmcnt(12)\n"
        "v_mfma_f32_32x32x16_bf16 v[0:15], v[64:67], a[32:35], v[0:15]\n"
        "ds_read_b64_tr_b16 v[84:85], v101 offset:20480\n"
        "ds_read_b64_tr_b16 v[86:87], v101 offset:20992\n"
        "s_waitcnt lgkmcnt(12)\n"
        "v_mfma_f32_32x32x16_bf16 v[16:31], v[68:71], a[32:35], v[16:31]\n"
        "ds_read_b128 a[44:47], v99\n"
        "ds_read_b64_tr_b16 v[88:89], v100 offset:22528\n"
        "ds_read_b64_tr_b16 v[90:91], v100 offset:23040\n"
        "s_waitcnt lgkmcnt(12)\n"
        "v_mfma_f32_32x32x16_bf16 v[0:15], v[72:75], a[36:39], v[0:15]\n"
        "ds_read_b64_tr_b16 v[92:93], v101 offset:22528\n"
        "ds_read_b64_tr_b16 v[94:95], v101 offset:23040\n"
        "s_waitcnt lgkmcnt(11)\n"
        "v_mfma_f32_32x32x16_bf16 v[16:31], v[76:79], a[36:39], v[16:31]\n"
        "v_add_u32 v96, 0x6000, v96\n"
        "v_add_u32 v97, 0x6000, v97\n"
        "v_add_u32 v98, 0x6000, v98\n"
        "v_add_u32 v99, 0x6000, v99\n"
        "v_add_u32 v100, 0x6000, v100\n"
        "v_add_u32 v101, 0x6000, v101\n"
        "LA3a%=:\n"
        "v_cmp_le_u32 vcc, 4, v104\n"
        "s_nop 1\n"
        "s_cmp_eq_u64 vcc, -1\n"
        "s_cbranch_scc1 LA3b%=\n"
        "ds_read_b32 v104, v103\n"
        "s_sub_u32 s59, s59, 1\n"
        "s_waitcnt lgkmcnt(0)\n"
        "s_cbranch_scc0 LA3a%=\n"
        "LA3b%=:\n"
        "ds_read_b128 a[48:51], v96\n"
        "ds_read_b64_tr_b16 v[64:65], v100 offset:16384\n"
        "ds_read_b64_tr_b16 v[66:67], v100 offset:16896\n"
        "s_waitcnt lgkmcnt(10)\n"
        "v_mfma_f32_32x32x16_bf16 v[0:15], v[80:83], a[40:43], v[0:15]\n"
        "ds_read_b64_tr_b16 v[68:69], v101 offset:16384\n"
        "ds_read_b64_tr_b16 v[70:71], v101 offset:16896\n"
        "s_waitcnt lgkmcnt(10)\n"
        "v_mfma_f32_32x32x16_bf16 v[16:31], v[84:87], a[40:43], v[16:31]\n"
        "ds_read_b128 a[52:55], v97\n"
        "ds_read_b64_tr_b16 v[72:73], v100 offset:18432\n"
        "ds_read_b64_tr_b16 v[74:75], v100 offset:18944\n"
        "ds_read_b32 v104, v103\n"
        "s_waitcnt lgkmcnt(11)\n"
        "v_mfma_f32_32x32x16_bf16 v[0:15], v[88:91], a[44:47], v[0:15]\n"
        "ds_read_b64_tr_b16 v[76:77], v101 offset:18432\n"
        "ds_read_b64_tr_b16 v[78:79], v101 offset:18944\n"
        "s_waitcnt lgkmcnt(11)\n"
        "v_mfma_f32_32x32x16_bf16 v[16:31], v[92:95], a[44:47], v[16:31]\n"
        "v_mov_b32 v106, 3\n"
        "s_mov_b64 exec, 1\n"
        "ds_write_b32 v105, v106\n"
        "s_mov_b64 exec, -1\n"
        "ds_read_b128 a[56:59], v98\n"
        "ds_read_b64_tr_b16 v[80:81], v100 offset:20480\n"
        "ds_read_b64_tr_b16 v[82:83], v100 offset:20992\n"
        "s_waitcnt lgkmcnt(12)\n"
        "v_mfma_f32_32x32x16_bf16 v[0:15], v[64:67], a[48:51], v[0:15]\n"
        "ds_read_b64_tr_b16 v[84:85], v101 offset:20480\n"
        "ds_read_b64_tr_b16 v[86:87], v101 offset:20992\n"
        "s_waitcnt lgkmcnt(12)\n"
        "v_mfma_f32_32x32x16_bf16 v[16:31], v[68:71], a[48:51], v[16:31]\n"
        "ds_read_b128 a[60:63], v99\n"
        "ds_read_b64_tr_b16 v[88:89], v100 offset:22528\n"
        "ds_read_b64_tr_b16 v[90:91], v100 offset:23040\n"
        "s_waitcnt lgkmcnt(12)\n"
        "v_mfma_f32_32x32x16_bf16 v[0:15], v[72:75], a[52:55], v[0:15]\n"
        "ds_read_b64_tr_b16 v[92:93], v101 offset:22528\n"
        "ds_read_b64_tr_b16 v[94:95], v101 offset:23040\n"
        "s_waitcnt lgkmcnt(11)\n"
        "v_mfma_f32_32x32x16_bf16 v[16:31], v[76:79], a[52:55], v[16:31]\n"
        "v_add_u32 v96, 0x6000, v96\n"
        "v_add_u32 v97, 0x6000, v97\n"
        "v_add_u32 v98, 0x6000, v98\n"
        "v_add_u32 v99, 0x6000, v99\n"
        "v_add_u32 v100, 0x6000, v100\n"
        "v_add_u32 v101, 0x6000, v101\n"
        "LA4a%=:\n"
        "v_cmp_le_u32 vcc, 5, v104\n"
        "s_nop 1\n"
        "s_cmp_eq_u64 vcc, -1\n"
        "s_cbranch_scc1 LA4b%=\n"
        "ds_read_b32 v104, v103\n"
        "s_sub_u32 s59, s59, 1\n"
        "s_waitcnt lgkmcnt(0)\n"
        "s_cbranch_scc0 LA4a%=\n"
        "LA4b%=:\n"
        "ds_read_b128 a[64:67], v96\n"
        "ds_read_b64_tr_b16 v[64:65], v100 offset:16384\n"
        "ds_read_b64_tr_b16 v[66:67], v100 offset:16896\n"
        "s_waitcnt lgkmcnt(10)\n"
        "v_mfma_f32_32x32x16_bf16 v[0:15], v[80:83], a[56:59], v[0:15]\n"
        "ds_read_b64_tr_b16 v[68:69], v101 offset:16384\n"
        "ds_read_b64_tr_b16 v[70:71], v101 offset:16896\n"
        "s_waitcnt lgkmcnt(10)\n"
        "v_mfma_f32_32x32x16_bf16 v[16:31], v[84:87], a[56:59], v[16:31]\n"
        "ds_read_b128 a[68:71], v97\n"
        "ds_read_b64_tr_b16 v[72:73], v100 offset:18432\n"
        "ds_read_b64_tr_b16 v[74:75], v100 offset:18944\n"
        "ds_read_b32 v104, v103\n"
        "s_waitcnt lgkmcnt(11)\n"
        "v_mfma_f32_32x32x16_bf16 v[0:15], v[88:91], a[60:63], v[0:15]\n"
        "ds_read_b64_tr_b16 v[76:77], v101 offset:18432\n"
        "ds_read_b64_tr_b16 v[78:79], v101 offset:18944\n"
        "s_waitcnt lgkmcnt(11)\n"
        "v_mfma_f32_32x32x16_bf16 v[16:31], v[92:95], a[60:63], v[16:31]\n"
        "v_mov_b32 v106, 4\n"
        "s_mov_b64 exec, 1\n"
        "ds_write_b32 v105, v106\n"
        "s_mov_b64 exec, -1\n"
        "ds_read_b128 a[72:75], v98\n"
        "ds_read_b64_tr_b16 v[80:81], v100 offset:20480\n"
        "ds_read_b64_tr_b16 v[82:83], v100 offset:20992\n"
        "s_waitcnt lgkmcnt(12)\n"
        "v_mfma_f32_32x32x16_bf16 v[0:15], v[64:67], a[64:67], v[0:15]\n"
        "ds_read_b64_tr_b16 v[84:85], v101 offset:20480\n"
        "ds_read_b64_tr_b16 v[86:87], v101 offset:20992\n"
        "s_waitcnt lgkmcnt(12)\n"
        "v_mfma_f32_32x32x16_bf16 v[16:31], v[68:71], a[64:67], v[16:31]\n"
        "ds_read_b128 a[76:79], v99\n"
        "ds_read_b64_tr_b16 v[88:89], v100 offset:22528\n"
        "ds_read_b64_tr_b16 v[90:91], v100 offset:23040\n"
        "s_waitcnt lgkmcnt(12)\n"
        "v_mfma_f32_32x32x16_bf16 v[0:15], v[72:75], a[68:71], v[0:15]\n"
        "ds_read_b64_tr_b16 v[92:93], v101 offset:22528\n"
        "ds_read_b64_tr_b16 v[94:95], v101 offset:23040\n"
        "s_waitcnt lgkmcnt(11)\n"
        "v_mfma_f32_32x32x16_bf16 v[16:31], v[76:79], a[68:71], v[16:31]\n"
        "v_add_u32 v96, 0xfffe8000, v96\n"
        "v_add_u32 v97, 0xfffe8000, v97\n"
        "v_add_u32 v98, 0xfffe8000, v98\n"
        "v_add_u32 v99, 0xfffe8000, v99\n"
        "v_add_u32 v100, 0xfffe8000, v100\n"
        "v_add_u32 v101, 0xfffe8000, v101\n"
        "LA5a%=:\n"
        "v_cmp_le_u32 vcc, 6, v104\n"
        "s_nop 1\n"
        "s_cmp_eq_u64 vcc, -1\n"
        "s_cbranch_scc1 LA5b%=\n"
        "ds_read_b32 v104, v103\n"
        "s_sub_u32 s59, s59, 1\n"
        "s_waitcnt lgkmcnt(0)\n"
        "s_cbranch_scc0 LA5a%=\n"
        "LA5b%=:\n"
        "ds_read_b128 a[80:83], v96\n"
        "ds_read_b64_tr_b16 v[64:65], v100 offset:16384\n"
        "ds_read_b64_tr_b16 v[66:67], v100 offset:16896\n"
        "s_waitcnt lgkmcnt(10)\n"
        "v_mfma_f32_32x32x16_bf16 v[0:15], v[80:83], a[72:75], v[0:15]\n"
        "ds_read_b64_tr_b16 v[68:69], v101 offset:16384\n"
        "ds_read_b64_tr_b16 v[70:71], v101 offset:16896\n"
        "s_waitcnt lgkmcnt(10)\n"
        "v_mfma_f32_32x32x16_bf16 v[16:31], v[84:87], a[72:75], v[16:31]\n"
        "ds_read_b128 a[84:87], v97\n"
        "ds_read_b64_tr_b16 v[72:73], v100 offset:18432\n"
        "ds_read_b64_tr_b16 v[74:75], v100 offset:18944\n"
        "ds_read_b32 v104, v103\n"
        "s_waitcnt lgkmcnt(11)\n"
        "v_mfma_f32_32x32x16_bf16 v[0:15], v[88:91], a[76:79], v[0:15]\n"
        "ds_read_b64_tr_b16 v[76:77], v101 offset:18432\n"
        "ds_read_b64_tr_b16 v[78:79], v101 offset:18944\n"
        "s_waitcnt lgkmcnt(11)\n"
        "v_mfma_f32_32x32x16_bf16 v[16:31], v[92:95], a[76:79], v[16:31]\n"
        "v_mov_b32 v106, 5\n"
        "s_mov_b64 exec, 1\n"
        "ds_write_b32 v105, v106\n"
        "s_mov_b64 exec, -1\n"
        "ds_read_b128 a[88:91], v98\n"
        "ds_read_b64_tr_b16 v[80:81], v100 offset:20480\n"
        "ds_read_b64_tr_b16 v[82:83], v100 offset:20992\n"
        "s_waitcnt lgkmcnt(12)\n"
        "v_mfma_f32_32x32x16_bf16 v[0:15], v[64:67], a[80:83], v[0:15]\n"
        "s_waitcnt vmcnt(0)\n"
        "ds_read_b64_tr_b16 v[84:85], v101 offset:20480\n"
        "ds_read_b64_tr_b16 v[86:87], v101 offset:20992\n"
        "s_waitcnt lgkmcnt(12)\n"
        "v_mfma_f32_32x32x16_bf16 v[16:31], v[68:71], a[80:83], v[16:31]\n"
        "ds_read_b128 a[92:95], v99\n"
        "ds_read_b64_tr_b16 v[88:89], v100 offset:22528\n"
        "ds_read_b64_tr_b16 v[90:91], v100 offset:23040\n"
        "s_waitcnt lgkmcnt(12)\n"
        "v_mfma_f32_32x32x16_bf16 v[0:15], v[72:75], a[84:87], v[0:15]\n"
        "ds_read_b64_tr_b16 v[92:93], v101 offset:22528\n"
        "ds_read_b64_tr_b16 v[94:95], v101 offset:23040\n"
        "s_waitcnt lgkmcnt(11)\n"
        "v_mfma_f32_32x32x16_bf16 v[16:31], v[76:79], a[84:87], v[16:31]\n"
        "v_add_u32 v96, 0x6000, v96\n"
        "v_add_u32 v97, 0x6000, v97\n"
        "v_add_u32 v98, 0x6000, v98\n"
        "v_add_u32 v99, 0x6000, v99\n"
        "v_add_u32 v100, 0x6000, v100\n"
        "v_add_u32 v101, 0x6000, v101\n"
        "LA6a%=:\n"
        "v_cmp_le_u32 vcc, 7, v104\n"
        "s_nop 1\n"
        "s_cmp_eq_u64 vcc, -1\n"
        "s_cbranch_scc1 LA6b%=\n"
        "ds_read_b32 v104, v103\n"
        "s_sub_u32 s59, s59, 1\n"
        "s_waitcnt lgkmcnt(0)\n"
        "s_cbranch_scc0 LA6a%=\n"
        "LA6b%=:\n"
        "ds_read_b128 a[96:99], v96\n"
        "ds_read_b64_tr_b16 v[64:65], v100 offset:16384\n"
        "ds_read_b64_tr_b16 v[66:67], v100 offset:16896\n"
        "s_waitcnt lgkmcnt(10)\n"
        "v_mfma_f32_32x32x16_bf16 v[0:15], v[80:83], a[88:91], v[0:15]\n"
        "ds_read_b64_tr_b16 v[68:69], v101 offset:16384\n"
        "ds_read_b64_tr_b16 v[70:71], v101 offset:16896\n"
        "s_waitcnt lgkmcnt(10)\n"
        "v_mfma_f32_32x32x16_bf16 v[16:31], v[84:87], a[88:91], v[16:31]\n"
        "ds_read_b128 a[100:103], v97\n"
        "ds_read_b64_tr_b16 v[72:73], v100 offset:18432\n"
        "ds_read_b64_tr_b16 v[74:75], v100 offset:18944\n"
        "ds_read_b32 v104, v103\n"
        "s_waitcnt lgkmcnt(11)\n"
        "v_mfma_f32_32x32x16_bf16 v[0:15], v[88:91], a[92:95], v[0:15]\n"
        "ds_read_b64_tr_b16 v[76:77], v101 offset:18432\n"
        "ds_read_b64_tr_b16 v[78:79], v101 offset:18944\n"
        "s_waitcnt lgkmcnt(11)\n"
        "v_mfma_f32_32x32x16_bf16 v[16:31], v[92:95], a[92:95], v[16:31]\n"
        "v_mov_b32 v106, 6\n"
        "s_mov_b64 exec, 1\n"
        "ds_write_b32 v105, v106\n"
        "s_mov_b64 exec, -1\n"
        "ds_read_b128 a[104:107], v98\n"
        "ds_read_b64_tr_b16 v[80:81], v100 offset:20480\n"
        "ds_read_b64_tr_b16 v[82:83], v100 offset:20992\n"
        "s_waitcnt lgkmcnt(12)\n"
        "v_mfma_f32_32x32x16_bf16 v[0:15], v[64:67], a[96:99], v[0:15]\n"
        "ds_read_b64_tr_b16 v[84:85], v101 offset:20480\n"
        "ds_read_b64_tr_b16 v[86:87], v101 offset:20992\n"
        "s_waitcnt lgkmcnt(12)\n"
        "v_mfma_f32_32x32x16_bf16 v[16:31], v[68:71], a[96:99], v[16:31]\n"
        "ds_read_b128 a[108:111], v99\n"
        "ds_read_b64_tr_b16 v[88:89], v100 offset:22528\n"
        "ds_read_b64_tr_b16 v[90:91], v100 offset:23040\n"
        "s_waitcnt lgkmcnt(12)\n"
        "v_mfma_f32_32x32x16_bf16 v[0:15], v[72:75], a[100:103], v[0:15]\n"
        "ds_read_b64_tr_b16 v[92:93], v101 offset:22528\n"
        "ds_read_b64_tr_b16 v[94:95], v101 offset:23040\n"
        "s_waitcnt lgkmcnt(11)\n"
        "v_mfma_f32_32x32x16_bf16 v[16:31], v[76:79], a[100:103], v[16:31]\n"
        "v_add_u32 v96, 0x6000, v96\n"
        "v_add_u32 v97, 0x6000, v97\n"
        "v_add_u32 v98, 0x6000, v98\n"
        "v_add_u32 v99, 0x6000, v99\n"
        "v_add_u32 v100, 0x6000, v100\n"
        "v_add_u32 v101, 0x6000, v101\n"
        "LA7a%=:\n"
        "v_cmp_le_u32 vcc, 8, v104\n"
        "s_nop 1\n"
        "s_cmp_eq_u64 vcc, -1\n"
        "s_cbranch_scc1 LA7b%=\n"
        "ds_read_b32 v104, v103\n"
        "s_sub_u32 s59, s59, 1\n"
        "s_waitcnt lgkmcnt(0)\n"
        "s_cbranch_scc0 LA7a%=\n"
        "LA7b%=:\n"
        "ds_read_b128 a[112:115], v96\n"
        "ds_read_b64_tr_b16 v[64:65], v100 offset:16384\n"
        "ds_read_b64_tr_b16 v[66:67], v100 offset:16896\n"
        "s_waitcnt lgkmcnt(10)\n"
        "v_mfma_f32_32x32x16_bf16 v[0:15], v[80:83], a[104:107], v[0:15]\n"
        "ds_read_b64_tr_b16 v[68:69], v101 offset:16384\n"
        "ds_read_b64_tr_b16 v[70:71], v101 offset:16896\n"
        "s_waitcnt lgkmcnt(10)\n"
        "v_mfma_f32_32x32x16_bf16 v[16:31], v[84:87], a[104:107], v[16:31]\n"
        "ds_read_b128 a[116:119], v97\n"
        "ds_read_b64_tr_b16 v[72:73], v100 offset:18432\n"
        "ds_read_b64_tr_b16 v[74:75], v100 offset:18944\n"
        "s_waitcnt lgkmcnt(10)\n"
        "v_mfma_f32_32x32x16_bf16 v[0:15], v[88:91], a[108:111], v[0:15]\n"
        "ds_read_b64_tr_b16 v[76:77], v101 offset:18432\n"
        "ds_read_b64_tr_b16 v[78:79], v101 offset:18944\n"
        "s_waitcnt lgkmcnt(10)\n"
        "v_mfma_f32_32x32x16_bf16 v[16:31], v[92:95], a[108:111], v[16:31]\n"
        "v_mov_b32 v106, 7\n"
        "s_mov_b64 exec, 1\n"
        "ds_write_b32 v105, v106\n"
        "s_mov_b64 exec, -1\n"
        "ds_read_b128 a[120:123], v98\n"
        "ds_read_b64_tr_b16 v[80:81], v100 offset:20480\n"
        "ds_read_b64_tr_b16 v[82:83], v100 offset:20992\n"
        "s_waitcnt lgkmcnt(11)\n"
        "v_mfma_f32_32x32x16_bf16 v[0:15], v[64:67], a[112:115], v[0:15]\n"
        "ds_read_b64_tr_b16 v[84:85], v101 offset:20480\n"
        "ds_read_b64_tr_b16 v[86:87], v101 offset:20992\n"
        "s_waitcnt lgkmcnt(11)\n"
        "v_mfma_f32_32x32x16_bf16 v[16:31], v[68:71], a[112:115], v[16:31]\n"
        "ds_read_b128 a[124:127], v99\n"
        "ds_read_b64_tr_b16 v[88:89], v100 offset:22528\n"
        "ds_read_b64_tr_b16 v[90:91], v100 offset:23040\n"
        "s_waitcnt lgkmcnt(11)\n"
        "v_mfma_f32_32x32x16_bf16 v[0:15], v[72:75], a[116:119], v[0:15]\n"
        "ds_read_b64_tr_b16 v[92:93], v101 offset:22528\n"
        "ds_read_b64_tr_b16 v[94:95], v101 offset:23040\n"
        "s_waitcnt lgkmcnt(11)\n"
        "v_mfma_f32_32x32x16_bf16 v[16:31], v[76:79], a[116:119], v[16:31]\n"
        "s_waitcnt lgkmcnt(7)\n"
        "v_mfma_f32_32x32x16_bf16 v[0:15], v[80:83], a[120:123], v[0:15]\n"
        "s_waitcnt lgkmcnt(5)\n"
        "v_mfma_f32_32x32x16_bf16 v[16:31], v[84:87], a[120:123], v[16:31]\n"
        "s_waitcnt lgkmcnt(2)\n"
        "v_mfma_f32_32x32x16_bf16 v[0:15], v[88:91], a[124:127], v[0:15]\n"
        "s_waitcnt lgkmcnt(0)\n"
        "v_mfma_f32_32x32x16_bf16 v[16:31], v[92:95], a[124:127], v[16:31]\n"
        "v_mov_b32 v106, 8\n"
        "s_mov_b64 exec, 1\n"
        "ds_write_b32 v105, v106\n"
        "s_mov_b64 exec, -1\n"
        "s_nop 7\n"
        "s_nop 3\n"
                : "={v[0:15]}"(acc0), "={v[16:31]}"(acc1), "+{v96}"(x0), "+{v97}"(x1), "+{v98}"(x2), "+{v99}"(x3),
                  "+{v100}"(wa), "+{v101}"(wb)
                : "{v103}"(pl), "{v105}"(po + 16)
                : MEM, "scc", "vcc", "v64","v65","v66","v67","v68","v69","v70","v71","v72","v73","v74","v75","v76","v77","v78","v79","v80","v81","v82","v83","v84","v85","v86","v87","v88","v89","v90","v91","v92","v93","v94","v95", "v104", "v106", "s59");
        }
        {
            const int slot_r = par ? ((r & 19) | ((r & 4) << 1) | ((r & 8) >> 1)) : r;
            unsigned qk = (par ? WS_K : WS_Q) + ((ui * 4 * 64 + rtile) * 64 + h * 32 + slot_r) * 16;
            float kv = 0.f;
            f32x16 qkp;
            if (!par) {
#pragma unroll
                for (int i = 0; i < 16; i++) { acc0[i] *= 0.25f * LOG2E; acc1[i] *= 0.25f * LOG2E; }
            }
#pragma unroll
            for (int hh = 0; hh < 4; hh++) {
                const f32x16& a = hh < 2 ? acc0 : acc1;
#pragma unroll
                for (int i = 0; i < 4; i++) qkp[hh * 4 + i] = u2f(pk(a[(hh & 1) * 8 + 2 * i], a[(hh & 1) * 8 + 2 * i + 1]));
            }
            if (par) {
                float n[4];
#pragma unroll
                for (int hh = 0; hh < 4; hh++) {
                    const f32x16& a = hh < 2 ? acc0 : acc1;
                    n[hh] = 0.f;
#pragma unroll
                    for (int i = 0; i < 8; i++) n[hh] = __builtin_fmaf(a[(hh & 1) * 8 + i], a[(hh & 1) * 8 + i], n[hh]);
                }
                float m = __builtin_fmaxf(__builtin_fmaxf(n[0], n[1]), __builtin_fmaxf(n[2], n[3]));
                m = row_max16(m + swap_half(m));
                kv = __builtin_fmaxf(rdlane(m, 0), rdlane(m, 16)) * 1.01f;
            }
            const u32x2 fdat = {f2u(kv), tag};
            const unsigned foffs = SM_FA + (unsigned)((c16 * 64 + rtile) * 128 + (lane & 3) * 8);
            u32x4 y0, y1, y2, y3;
#pragma unroll 1
            for (int ps = 0; ps < 2; ps++) {
                const unsigned single = (unsigned)ps & ((unsigned)j4 >> 1), hi = single & (unsigned)j4;
                wait_ge(pl, uni(10u + 4u * ps + single));
                if (ps) __builtin_amdgcn_s_setprio(1);
                unsigned wa = (wrd + (ps ? 0u : 0x12000u)) ^ (hi * 64u), wb = wa ^ 64u;
                u32x4 bt = {0u, 0u, 0u, 0u};
                const u32x4 on = {0x3f80u, 0u, 0u, 0u};
                asm volatile(
        "s_cmp_eq_u32 s60, 0\n"
        "s_cbranch_scc1 LD%=\n"
        "ds_read_b64_tr_b16 v[64:65], v100 offset:0\n"
        "ds_read_b64_tr_b16 v[66:67], v100 offset:512\n"
        "ds_read_b64_tr_b16 v[68:69], v100 offset:2048\n"
        "ds_read_b64_tr_b16 v[70:71], v100 offset:2560\n"
        "ds_read_b64_tr_b16 v[72:73], v100 offset:4096\n"
        "ds_read_b64_tr_b16 v[74:75], v100 offset:4608\n"
        "ds_read_b64_tr_b16 v[76:77], v100 offset:6144\n"
        "ds_read_b64_tr_b16 v[78:79], v100 offset:6656\n"
        "ds_read_b64_tr_b16 v[80:81], v100 offset:8192\n"
        "ds_read_b64_tr_b16 v[82:83], v100 offset:8704\n"
        "ds_read_b64_tr_b16 v[84:85], v100 offset:10240\n"
        "ds_read_b64_tr_b16 v[86:87], v100 offset:10752\n"
        "ds_read_b64_tr_b16 v[88:89], v100 offset:12288\n"
        "ds_read_b64_tr_b16 v[90:91], v100 offset:12800\n"
        "v_mov_b32 v104, v112\n"
        "s_nop 1\n"
        "v_mfma_f32_32x32x16_bf16 v[0:15], v[104:107], v[108:111], 0\n"
        "s_waitcnt lgkmcnt(12)\n"
        "v_mfma_f32_32x32x16_bf16 v[0:15], v[64:67], a[0:3], v[0:15]\n"
        "ds_read_b64_tr_b16 v[92:93], v100 offset:14336\n"
        "ds_read_b64_tr_b16 v[94:95], v100 offset:14848\n"
        "s_waitcnt lgkmcnt(12)\n"
        "v_mfma_f32_32x32x16_bf16 v[0:15], v[68:71], a[4:7], v[0:15]\n"
        "ds_read_b64_tr_b16 v[64:65], v100 offset:16384\n"
        "ds_read_b64_tr_b16 v[66:67], v100 offset:16896\n"
        "s_waitcnt lgkmcnt(12)\n"
        "v_mfma_f32_32x32x16_bf16 v[0:15], v[72:75], a[8:11], v[0:15]\n"
        "ds_read_b64_tr_b16 v[68:69], v100 offset:18432\n"
        "ds_read_b64_tr_b16 v[70:71], v100 offset:18944\n"
        "s_waitcnt lgkmcnt(12)\n"
        "v_mfma_f32_32x32x16_bf16 v[0:15], v[76:79], a[12:15], v[0:15]\n"
        "ds_read_b64_tr_b16 v[72:73], v100 offset:20480\n"
        "ds_read_b64_tr_b16 v[74:75], v100 offset:20992\n"
        "s_waitcnt lgkmcnt(12)\n"
        "v_mfma_f32_32x32x16_bf16 v[0:15], v[80:83], a[16:19], v[0:15]\n"
        "ds_read_b64_tr_b16 v[76:77], v100 offset:22528\n"
        "ds_read_b64_tr_b16 v[78:79], v100 offset:23040\n"
        "s_waitcnt lgkmcnt(12)\n"
        "v_mfma_f32_32x32x16_bf16 v[0:15], v[84:87], a[20:23], v[0:15]\n"
        "ds_read_b64_tr_b16 v[80:81], v100 offset:24576\n"
        "ds_read_b64_tr_b16 v[82:83], v100 offset:25088\n"
        "s_waitcnt lgkmcnt(12)\n"
        "v_mfma_f32_32x32x16_bf16 v[0:15], v[88:91], a[24:27], v[0:15]\n"
        "ds_read_b64_tr_b16 v[84:85], v100 offset:26624\n"
        "ds_read_b64_tr_b16 v[86:87], v100 offset:27136\n"
        "s_waitcnt lgkmcnt(12)\n"
        "v_mfma_f32_32x32x16_bf16 v[0:15], v[92:95], a[28:31], v[0:15]\n"
        "ds_read_b64_tr_b16 v[88:89], v100 offset:28672\n"
        "ds_read_b64_tr_b16 v[90:91], v100 offset:29184\n"
        "s_waitcnt lgkmcnt(12)\n"
        "v_mfma_f32_32x32x16_bf16 v[0:15], v[64:67], a[32:35], v[0:15]\n"
        "ds_read_b64_tr_b16 v[92:93], v100 offset:30720\n"
        "ds_read_b64_tr_b16 v[94:95], v100 offset:31232\n"
        "s_waitcnt lgkmcnt(12)\n"
        "v_mfma_f32_32x32x16_bf16 v[0:15], v[68:71], a[36:39], v[0:15]\n"
        "ds_read_b64_tr_b16 v[64:65], v100 offset:32768\n"
        "ds_read_b64_tr_b16 v[66:67], v100 offset:33280\n"
        "s_waitcnt lgkmcnt(12)\n"
        "v_mfma_f32_32x32x16_bf16 v[0:15], v[72:75], a[40:43], v[0:15]\n"
        "ds_read_b64_tr_b16 v[68:69], v100 offset:34816\n"
        "ds_read_b64_tr_b16 v[70:71], v100 offset:35328\n"
        "s_waitcnt lgkmcnt(12)\n"
        "v_mfma_f32_32x32x16_bf16 v[0:15], v[76:79], a[44:47], v[0:15]\n"
        "ds_read_b64_tr_b16 v[72:73], v100 offset:36864\n"
        "ds_read_b64_tr_b16 v[74:75], v100 offset:37376\n"
        "s_waitcnt lgkmcnt(12)\n"
        "v_mfma_f32_32x32x16_bf16 v[0:15], v[80:83], a[48:51], v[0:15]\n"
        "ds_read_b64_tr_b16 v[76:77], v100 offset:38912\n"
        "ds_read_b64_tr_b16 v[78:79], v100 offset:39424\n"
        "s_waitcnt lgkmcnt(12)\n"
        "v_mfma_f32_32x32x16_bf16 v[0:15], v[84:87], a[52:55], v[0:15]\n"
        "ds_read_b64_tr_b16 v[80:81], v100 offset:40960\n"
        "ds_read_b64_tr_b16 v[82:83], v100 offset:41472\n"
        "s_waitcnt lgkmcnt(12)\n"
        "v_mfma_f32_32x32x16_bf16 v[0:15], v[88:91], a[56:59], v[0:15]\n"
        "ds_read_b64_tr_b16 v[84:85], v100 offset:43008\n"
        "ds_read_b64_tr_b16 v[86:87], v100 offset:43520\n"
        "s_waitcnt lgkmcnt(12)\n"
        "v_mfma_f32_32x32x16_bf16 v[0:15], v[92:95], a[60:63], v[0:15]\n"
        "ds_read_b64_tr_b16 v[88:89], v100 offset:45056\n"
        "ds_read_b64_tr_b16 v[90:91], v100 offset:45568\n"
        "s_waitcnt lgkmcnt(12)\n"
        "v_mfma_f32_32x32x16_bf16 v[0:15], v[64:67], a[64:67], v[0:15]\n"
        "ds_read_b64_tr_b16 v[92:93], v100 offset:47104\n"
        "ds_read_b64_tr_b16 v[94:95], v100 offset:47616\n"
        "s_waitcnt lgkmcnt(12)\n"
        "v_mfma_f32_32x32x16_bf16 v[0:15], v[68:71], a[68:71], v[0:15]\n"
        "v_add_u32 v100, s56, v100\n"
        "ds_read_b64_tr_b16 v[64:65], v100 offset:49152\n"
        "ds_read_b64_tr_b16 v[66:67], v100 offset:49664\n"
        "s_waitcnt lgkmcnt(12)\n"
        "v_mfma_f32_32x32x16_bf16 v[0:15], v[72:75], a[72:75], v[0:15]\n"
        "ds_read_b64_tr_b16 v[68:69], v100 offset:51200\n"
        "ds_read_b64_tr_b16 v[70:71], v100 offset:51712\n"
        "s_waitcnt lgkmcnt(12)\n"
        "v_mfma_f32_32x32x16_bf16 v[0:15], v[76:79], a[76:79], v[0:15]\n"
        "ds_read_b64_tr_b16 v[72:73], v100 offset:53248\n"
        "ds_read_b64_tr_b16 v[74:75], v100 offset:53760\n"
        "s_waitcnt lgkmcnt(12)\n"
        "v_mfma_f32_32x32x16_bf16 v[0:15], v[80:83], a[80:83], v[0:15]\n"
        "ds_read_b64_tr_b16 v[76:77], v100 offset:55296\n"
        "ds_read_b64_tr_b16 v[78:79], v100 offset:55808\n"
        "s_waitcnt lgkmcnt(12)\n"
        "v_mfma_f32_32x32x16_bf16 v[0:15], v[84:87], a[84:87], v[0:15]\n"
        "ds_read_b64_tr_b16 v[80:81], v100 offset:57344\n"
        "ds_read_b64_tr_b16 v[82:83], v100 offset:57856\n"
        "s_waitcnt lgkmcnt(12)\n"
        "v_mfma_f32_32x32x16_bf16 v[0:15], v[88:91], a[88:91], v[0:15]\n"
        "ds_read_b64_tr_b16 v[84:85], v100 offset:59392\n"
        "ds_read_b64_tr_b16 v[86:87], v100 offset:59904\n"
        "s_waitcnt lgkmcnt(12)\n"
        "v_mfma_f32_32x32x16_bf16 v[0:15], v[92:95], a[92:95], v[0:15]\n"
        "ds_read_b64_tr_b16 v[88:89], v100 offset:61440\n"
        "ds_read_b64_tr_b16 v[90:91], v100 offset:61952\n"
        "s_waitcnt lgkmcnt(12)\n"
        "v_mfma_f32_32x32x16_bf16 v[0:15], v[64:67], a[96:99], v[0:15]\n"
        "ds_read_b64_tr_b16 v[92:93], v100 offset:63488\n"
        "ds_read_b64_tr_b16 v[94:95], v100 offset:64000\n"
        "s_waitcnt lgkmcnt(12)\n"
        "v_mfma_f32_32x32x16_bf16 v[0:15], v[68:71], a[100:103], v[0:15]\n"
        "s_waitcnt lgkmcnt(10)\n"
        "v_mfma_f32_32x32x16_bf16 v[0:15], v[72:75], a[104:107], v[0:15]\n"
        "s_waitcnt lgkmcnt(8)\n"
        "v_mfma_f32_32x32x16_bf16 v[0:15], v[76:79], a[108:111], v[0:15]\n"
        "s_waitcnt lgkmcnt(6)\n"
        "v_mfma_f32_32x32x16_bf16 v[0:15], v[80:83], a[112:115], v[0:15]\n"
        "s_waitcnt lgkmcnt(4)\n"
        "v_mfma_f32_32x32x16_bf16 v[0:15], v[84:87], a[116:119], v[0:15]\n"
        "s_waitcnt lgkmcnt(2)\n"
        "v_mfma_f32_32x32x16_bf16 v[0:15], v[88:91], a[120:123], v[0:15]\n"
        "s_waitcnt lgkmcnt(0)\n"
        "v_mfma_f32_32x32x16_bf16 v[0:15], v[92:95], a[124:127], v[0:15]\n"
        "s_nop 7\n"
        "s_nop 4\n"
        "s_branch LE%=\n"
        "LD%=:\n"
        "s_mov_b32 s59, 0x100000\n"
        "ds_read_b64_tr_b16 v[64:65], v100 offset:0\n"
        "ds_read_b64_tr_b16 v[66:67], v100 offset:512\n"
        "s_cmp_lg_u32 s57, 11\n"
        "s_cbranch_scc1 LS0_%=\n"
        "global_store_dwordx4 v96, v[48:51], s[48:49] sc1\n"
        "v_add_u32 v96, 0x10000, v96\n"
        "LS0_%=:\n"
        "ds_read_b64_tr_b16 v[68:69], v101 offset:0\n"
        "ds_read_b64_tr_b16 v[70:71], v101 offset:512\n"
        "ds_read_b64_tr_b16 v[72:73], v100 offset:2048\n"
        "ds_read_b64_tr_b16 v[74:75], v100 offset:2560\n"
        "s_cmp_lg_u32 s57, 11\n"
        "s_cbranch_scc1 LS1_%=\n"
        "global_store_dwordx4 v96, v[52:55], s[48:49] sc1\n"
        "v_add_u32 v96, 0x10000, v96\n"
        "LS1_%=:\n"
        "ds_read_b64_tr_b16 v[76:77], v101 offset:2048\n"
        "ds_read_b64_tr_b16 v[78:79], v101 offset:2560\n"
        "ds_read_b64_tr_b16 v[80:81], v100 offset:4096\n"
        "ds_read_b64_tr_b16 v[82:83], v100 offset:4608\n"
        "s_cmp_lg_u32 s57, 11\n"
        "s_cbranch_scc1 LS2_%=\n"
        "global_store_dwordx4 v96, v[56:59], s[48:49] sc1\n"
        "v_add_u32 v96, 0x10000, v96\n"
        "LS2_%=:\n"
        "ds_read_b64_tr_b16 v[84:85], v101 offset:4096\n"
        "ds_read_b64_tr_b16 v[86:87], v101 offset:4608\n"
        "ds_read_b64_tr_b16 v[88:89], v100 offset:6144\n"
        "ds_read_b64_tr_b16 v[90:91], v100 offset:6656\n"
        "s_cmp_lg_u32 s57, 11\n"
        "s_cbranch_scc1 LS3_%=\n"
        "global_store_dwordx4 v96, v[60:63], s[48:49] sc1\n"
        "v_add_u32 v96, 0x10000, v96\n"
        "LS3_%=:\n"
        "v_mov_b32 v104, v112\n"
        "s_nop 1\n"
        "v_mfma_f32_32x32x16_bf16 v[0:15], v[104:107], v[108:111], 0\n"
        "v_mov_b32 v104, v113\n"
        "s_nop 1\n"
        "v_mfma_f32_32x32x16_bf16 v[16:31], v[104:107], v[108:111], 0\n"
        "s_waitcnt lgkmcnt(12)\n"
        "v_mfma_f32_32x32x16_bf16 v[0:15], v[64:67], a[0:3], v[0:15]\n"
        "ds_read_b64_tr_b16 v[92:93], v101 offset:6144\n"
        "ds_read_b64_tr_b16 v[94:95], v101 offset:6656\n"
        "s_waitcnt lgkmcnt(12)\n"
        "v_mfma_f32_32x32x16_bf16 v[16:31], v[68:71], a[0:3], v[16:31]\n"
        "ds_read_b64_tr_b16 v[64:65], v100 offset:8192\n"
        "ds_read_b64_tr_b16 v[66:67], v100 offset:8704\n"
        "s_waitcnt lgkmcnt(12)\n"
        "v_mfma_f32_32x32x16_bf16 v[0:15], v[72:75], a[4:7], v[0:15]\n"
        "ds_read_b64_tr_b16 v[68:69], v101 offset:8192\n"
        "ds_read_b64_tr_b16 v[70:71], v101 offset:8704\n"
        "s_waitcnt lgkmcnt(12)\n"
        "v_mfma_f32_32x32x16_bf16 v[16:31], v[76:79], a[4:7], v[16:31]\n"
        "ds_read_b64_tr_b16 v[72:73], v100 offset:10240\n"
        "ds_read_b64_tr_b16 v[74:75], v100 offset:10752\n"
        "s_waitcnt lgkmcnt(12)\n"
        "v_mfma_f32_32x32x16_bf16 v[0:15], v[80:83], a[8:11], v[0:15]\n"
        "ds_read_b64_tr_b16 v[76:77], v101 offset:10240\n"
        "ds_read_b64_tr_b16 v[78:79], v101 offset:10752\n"
        "s_waitcnt lgkmcnt(12)\n"
        "v_mfma_f32_32x32x16_bf16 v[16:31], v[84:87], a[8:11], v[16:31]\n"
        "ds_read_b64_tr_b16 v[80:81], v100 offset:12288\n"
        "ds_read_b64_tr_b16 v[82:83], v100 offset:12800\n"
        "s_waitcnt lgkmcnt(12)\n"
        "v_mfma_f32_32x32x16_bf16 v[0:15], v[88:91], a[12:15], v[0:15]\n"
        "ds_read_b64_tr_b16 v[84:85], v101 offset:12288\n"
        "ds_read_b64_tr_b16 v[86:87], v101 offset:12800\n"
        "s_waitcnt lgkmcnt(12)\n"
        "v_mfma_f32_32x32x16_bf16 v[16:31], v[92:95], a[12:15], v[16:31]\n"
        "ds_read_b64_tr_b16 v[88:89], v100 offset:14336\n"
        "ds_read_b64_tr_b16 v[90:91], v100 offset:14848\n"
        "s_waitcnt lgkmcnt(12)\n"
        "v_mfma_f32_32x32x16_bf16 v[0:15], v[64:67], a[16:19], v[0:15]\n"
        "ds_read_b64_tr_b16 v[92:93], v101 offset:14336\n"
        "ds_read_b64_tr_b16 v[94:95], v101 offset:14848\n"
        "s_waitcnt lgkmcnt(12)\n"
        "v_mfma_f32_32x32x16_bf16 v[16:31], v[68:71], a[16:19], v[16:31]\n"
        "ds_read_b64_tr_b16 v[64:65], v100 offset:16384\n"
        "ds_read_b64_tr_b16 v[66:67], v100 offset:16896\n"
        "ds_read_b32 v102, v103\n"
        "s_waitcnt lgkmcnt(13)\n"
        "v_mfma_f32_32x32x16_bf16 v[0:15], v[72:75], a[20:23], v[0:15]\n"
        "ds_read_b64_tr_b16 v[68:69], v101 offset:16384\n"
        "ds_read_b64_tr_b16 v[70:71], v101 offset:16896\n"
        "s_waitcnt lgkmcnt(13)\n"
        "v_mfma_f32_32x32x16_bf16 v[16:31], v[76:79], a[20:23], v[16:31]\n"
        "ds_read_b64_tr_b16 v[72:73], v100 offset:18432\n"
        "ds_read_b64_tr_b16 v[74:75], v100 offset:18944\n"
        "s_waitcnt lgkmcnt(13)\n"
        "v_mfma_f32_32x32x16_bf16 v[0:15], v[80:83], a[24:27], v[0:15]\n"
        "ds_read_b64_tr_b16 v[76:77], v101 offset:18432\n"
        "ds_read_b64_tr_b16 v[78:79], v101 offset:18944\n"
        "s_waitcnt lgkmcnt(13)\n"
        "v_mfma_f32_32x32x16_bf16 v[16:31], v[84:87], a[24:27], v[16:31]\n"
        "ds_read_b64_tr_b16 v[80:81], v100 offset:20480\n"
        "ds_read_b64_tr_b16 v[82:83], v100 offset:20992\n"
        "s_waitcnt lgkmcnt(13)\n"
        "v_mfma_f32_32x32x16_bf16 v[0:15], v[88:91], a[28:31], v[0:15]\n"
        "ds_read_b64_tr_b16 v[84:85], v101 offset:20480\n"
        "ds_read_b64_tr_b16 v[86:87], v101 offset:20992\n"
        "s_waitcnt lgkmcnt(13)\n"
        "v_mfma_f32_32x32x16_bf16 v[16:31], v[92:95], a[28:31], v[16:31]\n"
        "ds_read_b64_tr_b16 v[88:89], v100 offset:22528\n"
        "ds_read_b64_tr_b16 v[90:91], v100 offset:23040\n"
        "s_waitcnt lgkmcnt(13)\n"
        "v_mfma_f32_32x32x16_bf16 v[0:15], v[64:67], a[32:35], v[0:15]\n"
        "ds_read_b64_tr_b16 v[92:93], v101 offset:22528\n"
        "ds_read_b64_tr_b16 v[94:95], v101 offset:23040\n"
        "s_waitcnt lgkmcnt(12)\n"
        "v_mfma_f32_32x32x16_bf16 v[16:31], v[68:71], a[32:35], v[16:31]\n"
        "LBa%=:\n"
        "v_cmp_le_u32 vcc, s57, v102\n"
        "s_nop 1\n"
        "s_cmp_eq_u64 vcc, -1\n"
        "s_cbranch_scc1 LBb%=\n"
        "ds_read_b32 v102, v103\n"
        "s_sub_u32 s59, s59, 1\n"
        "s_waitcnt lgkmcnt(0)\n"
        "s_cbranch_scc0 LBa%=\n"
        "LBb%=:\n"
        "ds_read_b64_tr_b16 v[64:65], v100 offset:24576\n"
        "ds_read_b64_tr_b16 v[66:67], v100 offset:25088\n"
        "s_waitcnt lgkmcnt(12)\n"
        "v_mfma_f32_32x32x16_bf16 v[0:15], v[72:75], a[36:39], v[0:15]\n"
        "ds_read_b64_tr_b16 v[68:69], v101 offset:24576\n"
        "ds_read_b64_tr_b16 v[70:71], v101 offset:25088\n"
        "s_waitcnt lgkmcnt(12)\n"
        "v_mfma_f32_32x32x16_bf16 v[16:31], v[76:79], a[36:39], v[16:31]\n"
        "ds_read_b64_tr_b16 v[72:73], v100 offset:26624\n"
        "ds_read_b64_tr_b16 v[74:75], v100 offset:27136\n"
        "s_cmp_lg_u32 s57, 11\n"
        "s_cbranch_scc1 LV%=\n"
        "s_waitcnt vmcnt(0)\n"
        "s_mov_b64 exec, 15\n"
        "global_store_dwordx2 v97, v[114:115], s[46:47] sc1\n"
        "s_mov_b64 exec, -1\n"
        "LV%=:\n"
        "s_waitcnt lgkmcnt(12)\n"
        "v_mfma_f32_32x32x16_bf16 v[0:15], v[80:83], a[40:43], v[0:15]\n"
        "ds_read_b64_tr_b16 v[76:77], v101 offset:26624\n"
        "ds_read_b64_tr_b16 v[78:79], v101 offset:27136\n"
        "s_waitcnt lgkmcnt(12)\n"
        "v_mfma_f32_32x32x16_bf16 v[16:31], v[84:87], a[40:43], v[16:31]\n"
        "ds_read_b64_tr_b16 v[80:81], v100 offset:28672\n"
        "ds_read_b64_tr_b16 v[82:83], v100 offset:29184\n"
        "s_waitcnt lgkmcnt(12)\n"
        "v_mfma_f32_32x32x16_bf16 v[0:15], v[88:91], a[44:47], v[0:15]\n"
        "ds_read_b64_tr_b16 v[84:85], v101 offset:28672\n"
        "ds_read_b64_tr_b16 v[86:87], v101 offset:29184\n"
        "s_waitcnt lgkmcnt(12)\n"
        "v_mfma_f32_32x32x16_bf16 v[16:31], v[92:95], a[44:47], v[16:31]\n"
        "ds_read_b64_tr_b16 v[88:89], v100 offset:30720\n"
        "ds_read_b64_tr_b16 v[90:91], v100 offset:31232\n"
        "s_waitcnt lgkmcnt(12)\n"
        "v_mfma_f32_32x32x16_bf16 v[0:15], v[64:67], a[48:51], v[0:15]\n"
        "ds_read_b64_tr_b16 v[92:93], v101 offset:30720\n"
        "ds_read_b64_tr_b16 v[94:95], v101 offset:31232\n"
        "s_waitcnt lgkmcnt(12)\n"
        "v_mfma_f32_32x32x16_bf16 v[16:31], v[68:71], a[48:51], v[16:31]\n"
        "ds_read_b64_tr_b16 v[64:65], v100 offset:32768\n"
        "ds_read_b64_tr_b16 v[66:67], v100 offset:33280\n"
        "s_waitcnt lgkmcnt(12)\n"
        "v_mfma_f32_32x32x16_bf16 v[0:15], v[72:75], a[52:55], v[0:15]\n"
        "ds_read_b64_tr_b16 v[68:69], v101 offset:32768\n"
        "ds_read_b64_tr_b16 v[70:71], v101 offset:33280\n"
        "s_waitcnt lgkmcnt(12)\n"
        "v_mfma_f32_32x32x16_bf16 v[16:31], v[76:79], a[52:55], v[16:31]\n"
        "ds_read_b64_tr_b16 v[72:73], v100 offset:34816\n"
        "ds_read_b64_tr_b16 v[74:75], v100 offset:35328\n"
        "s_waitcnt lgkmcnt(12)\n"
        "v_mfma_f32_32x32x16_bf16 v[0:15], v[80:83], a[56:59], v[0:15]\n"
        "ds_read_b64_tr_b16 v[76:77], v101 offset:34816\n"
        "ds_read_b64_tr_b16 v[78:79], v101 offset:35328\n"
        "s_waitcnt lgkmcnt(12)\n"
        "v_mfma_f32_32x32x16_bf16 v[16:31], v[84:87], a[56:59], v[16:31]\n"
        "ds_read_b64_tr_b16 v[80:81], v100 offset:36864\n"
        "ds_read_b64_tr_b16 v[82:83], v100 offset:37376\n"
        "s_waitcnt lgkmcnt(12)\n"
        "v_mfma_f32_32x32x16_bf16 v[0:15], v[88:91], a[60:63], v[0:15]\n"
        "ds_read_b64_tr_b16 v[84:85], v101 offset:36864\n"
        "ds_read_b64_tr_b16 v[86:87], v101 offset:37376\n"
        "s_waitcnt lgkmcnt(12)\n"
        "v_mfma_f32_32x32x16_bf16 v[16:31], v[92:95], a[60:63], v[16:31]\n"
        "ds_read_b64_tr_b16 v[88:89], v100 offset:38912\n"
        "ds_read_b64_tr_b16 v[90:91], v100 offset:39424\n"
        "s_waitcnt lgkmcnt(12)\n"
        "v_mfma_f32_32x32x16_bf16 v[0:15], v[64:67], a[64:67], v[0:15]\n"
        "ds_read_b64_tr_b16 v[92:93], v101 offset:38912\n"
        "ds_read_b64_tr_b16 v[94:95], v101 offset:39424\n"
        "s_waitcnt lgkmcnt(12)\n"
        "v_mfma_f32_32x32x16_bf16 v[16:31], v[68:71], a[64:67], v[16:31]\n"
        "ds_read_b64_tr_b16 v[64:65], v100 offset:40960\n"
        "ds_read_b64_tr_b16 v[66:67], v100 offset:41472\n"
        "ds_read_b32 v102, v103\n"
        "s_waitcnt lgkmcnt(13)\n"
        "v_mfma_f32_32x32x16_bf16 v[0:15], v[72:75], a[68:71], v[0:15]\n"
        "ds_read_b64_tr_b16 v[68:69], v101 offset:40960\n"
        "ds_read_b64_tr_b16 v[70:71], v101 offset:41472\n"
        "s_waitcnt lgkmcnt(13)\n"
        "v_mfma_f32_32x32x16_bf16 v[16:31], v[76:79], a[68:71], v[16:31]\n"
        "ds_read_b64_tr_b16 v[72:73], v100 offset:43008\n"
        "ds_read_b64_tr_b16 v[74:75], v100 offset:43520\n"
        "s_waitcnt lgkmcnt(13)\n"
        "v_mfma_f32_32x32x16_bf16 v[0:15], v[80:83], a[72:75], v[0:15]\n"
        "ds_read_b64_tr_b16 v[76:77], v101 offset:43008\n"
        "ds_read_b64_tr_b16 v[78:79], v101 offset:43520\n"
        "s_waitcnt lgkmcnt(13)\n"
        "v_mfma_f32_32x32x16_bf16 v[16:31], v[84:87], a[72:75], v[16:31]\n"
        "ds_read_b64_tr_b16 v[80:81], v100 offset:45056\n"
        "ds_read_b64_tr_b16 v[82:83], v100 offset:45568\n"
        "s_waitcnt lgkmcnt(13)\n"
        "v_mfma_f32_32x32x16_bf16 v[0:15], v[88:91], a[76:79], v[0:15]\n"
        "ds_read_b64_tr_b16 v[84:85], v101 offset:45056\n"
        "ds_read_b64_tr_b16 v[86:87], v101 offset:45568\n"
        "s_waitcnt lgkmcnt(13)\n"
        "v_mfma_f32_32x32x16_bf16 v[16:31], v[92:95], a[76:79], v[16:31]\n"
        "ds_read_b64_tr_b16 v[88:89], v100 offset:47104\n"
        "ds_read_b64_tr_b16 v[90:91], v100 offset:47616\n"
        "s_waitcnt lgkmcnt(13)\n"
        "v_mfma_f32_32x32x16_bf16 v[0:15], v[64:67], a[80:83], v[0:15]\n"
        "ds_read_b64_tr_b16 v[92:93], v101 offset:47104\n"
        "ds_read_b64_tr_b16 v[94:95], v101 offset:47616\n"
        "s_waitcnt lgkmcnt(12)\n"
        "v_mfma_f32_32x32x16_bf16 v[16:31], v[68:71], a[80:83], v[16:31]\n"
        "LCa%=:\n"
        "v_cmp_le_u32 vcc, s58, v102\n"
        "s_nop 1\n"
        "s_cmp_eq_u64 vcc, -1\n"
        "s_cbranch_scc1 LCb%=\n"
        "ds_read_b32 v102, v103\n"
        "s_sub_u32 s59, s59, 1\n"
        "s_waitcnt lgkmcnt(0)\n"
        "s_cbranch_scc0 LCa%=\n"
        "LCb%=:\n"
        "v_add_u32 v100, s56, v100\n"
        "v_add_u32 v101, s56, v101\n"
        "ds_read_b64_tr_b16 v[64:65], v100 offset:49152\n"
        "ds_read_b64_tr_b16 v[66:67], v100 offset:49664\n"
        "s_waitcnt lgkmcnt(12)\n"
        "v_mfma_f32_32x32x16_bf16 v[0:15], v[72:75], a[84:87], v[0:15]\n"
        "ds_read_b64_tr_b16 v[68:69], v101 offset:49152\n"
        "ds_read_b64_tr_b16 v[70:71], v101 offset:49664\n"
        "s_waitcnt lgkmcnt(12)\n"
        "v_mfma_f32_32x32x16_bf16 v[16:31], v[76:79], a[84:87], v[16:31]\n"
        "ds_read_b64_tr_b16 v[72:73], v100 offset:51200\n"
        "ds_read_b64_tr_b16 v[74:75], v100 offset:51712\n"
        "s_waitcnt lgkmcnt(12)\n"
        "v_mfma_f32_32x32x16_bf16 v[0:15], v[80:83], a[88:91], v[0:15]\n"
        "ds_read_b64_tr_b16 v[76:77], v101 offset:51200\n"
        "ds_read_b64_tr_b16 v[78:79], v101 offset:51712\n"
        "s_waitcnt lgkmcnt(12)\n"
        "v_mfma_f32_32x32x16_bf16 v[16:31], v[84:87], a[88:91], v[16:31]\n"
        "ds_read_b64_tr_b16 v[80:81], v100 offset:53248\n"
        "ds_read_b64_tr_b16 v[82:83], v100 offset:53760\n"
        "s_waitcnt lgkmcnt(12)\n"
        "v_mfma_f32_32x32x16_bf16 v[0:15], v[88:91], a[92:95], v[0:15]\n"
        "ds_read_b64_tr_b16 v[84:85], v101 offset:53248\n"
        "ds_read_b64_tr_b16 v[86:87], v101 offset:53760\n"
        "s_waitcnt lgkmcnt(12)\n"
        "v_mfma_f32_32x32x16_bf16 v[16:31], v[92:95], a[92:95], v[16:31]\n"
        "ds_read_b64_tr_b16 v[88:89], v100 offset:55296\n"
        "ds_read_b64_tr_b16 v[90:91], v100 offset:55808\n"
        "s_waitcnt lgkmcnt(12)\n"
        "v_mfma_f32_32x32x16_bf16 v[0:15], v[64:67], a[96:99], v[0:15]\n"
        "ds_read_b64_tr_b16 v[92:93], v101 offset:55296\n"
        "ds_read_b64_tr_b16 v[94:95], v101 offset:55808\n"
        "s_waitcnt lgkmcnt(12)\n"
        "v_mfma_f32_32x32x16_bf16 v[16:31], v[68:71], a[96:99], v[16:31]\n"
        "ds_read_b64_tr_b16 v[64:65], v100 offset:57344\n"
        "ds_read_b64_tr_b16 v[66:67], v100 offset:57856\n"
        "s_waitcnt lgkmcnt(12)\n"
        "v_mfma_f32_32x32x16_bf16 v[0:15], v[72:75], a[100:103], v[0:15]\n"
        "ds_read_b64_tr_b16 v[68:69], v101 offset:57344\n"
        "ds_read_b64_tr_b16 v[70:71], v101 offset:57856\n"
        "s_waitcnt lgkmcnt(12)\n"
        "v_mfma_f32_32x32x16_bf16 v[16:31], v[76:79], a[100:103], v[16:31]\n"
        "ds_read_b64_tr_b16 v[72:73], v100 offset:59392\n"
        "ds_read_b64_tr_b16 v[74:75], v100 offset:59904\n"
        "s_waitcnt lgkmcnt(12)\n"
        "v_mfma_f32_32x32x16_bf16 v[0:15], v[80:83], a[104:107], v[0:15]\n"
        "ds_read_b64_tr_b16 v[76:77], v101 offset:59392\n"
        "ds_read_b64_tr_b16 v[78:79], v101 offset:59904\n"
        "s_waitcnt lgkmcnt(12)\n"
        "v_mfma_f32_32x32x16_bf16 v[16:31], v[84:87], a[104:107], v[16:31]\n"
        "ds_read_b64_tr_b16 v[80:81], v100 offset:61440\n"
        "ds_read_b64_tr_b16 v[82:83], v100 offset:61952\n"
        "s_waitcnt lgkmcnt(12)\n"
        "v_mfma_f32_32x32x16_bf16 v[0:15], v[88:91], a[108:111], v[0:15]\n"
        "ds_read_b64_tr_b16 v[84:85], v101 offset:61440\n"
        "ds_read_b64_tr_b16 v[86:87], v101 offset:61952\n"
        "s_waitcnt lgkmcnt(12)\n"
        "v_mfma_f32_32x32x16_bf16 v[16:31], v[92:95], a[108:111], v[16:31]\n"
        "ds_read_b64_tr_b16 v[88:89], v100 offset:63488\n"
        "ds_read_b64_tr_b16 v[90:91], v100 offset:64000\n"
        "s_waitcnt lgkmcnt(12)\n"
        "v_mfma_f32_32x32x16_bf16 v[0:15], v[64:67], a[112:115], v[0:15]\n"
        "ds_read_b64_tr_b16 v[92:93], v101 offset:63488\n"
        "ds_read_b64_tr_b16 v[94:95], v101 offset:64000\n"
        "s_waitcnt lgkmcnt(12)\n"
        "v_mfma_f32_32x32x16_bf16 v[16:31], v[68:71], a[112:115], v[16:31]\n"
        "s_waitcnt lgkmcnt(10)\n"
        "v_mfma_f32_32x32x16_bf16 v[0:15], v[72:75], a[116:119], v[0:15]\n"
        "s_waitcnt lgkmcnt(8)\n"
        "v_mfma_f32_32x32x16_bf16 v[16:31], v[76:79], a[116:119], v[16:31]\n"
        "s_waitcnt lgkmcnt(6)\n"
        "v_mfma_f32_32x32x16_bf16 v[0:15], v[80:83], a[120:123], v[0:15]\n"
        "s_waitcnt lgkmcnt(4)\n"
        "v_mfma_f32_32x32x16_bf16 v[16:31], v[84:87], a[120:123], v[16:31]\n"
        "s_waitcnt lgkmcnt(2)\n"
        "v_mfma_f32_32x32x16_bf16 v[0:15], v[88:91], a[124:127], v[0:15]\n"
        "s_waitcnt lgkmcnt(0)\n"
        "v_mfma_f32_32x32x16_bf16 v[16:31], v[92:95], a[124:127], v[16:31]\n"
        "s_nop 7\n"
        "s_nop 4\n"
        "LE%=:\n"
                    : "={v[0:15]}"(acc0), "={v[16:31]}"(acc1), "+{v[104:107]}"(bt), "+{v100}"(wa), "+{v101}"(wb), "+{v96}"(qk)
                    : "{v[48:63]}"(qkp), "{s[48:49]}"(ws), "{v[108:111]}"(on), "{s60}"(single), "{v112}"(h ? 0u : (ps ? (hi ? bias[3] : bias[2]) : bias[0])), "{v113}"(h ? 0u : (ps ? bias[3] : bias[1])),
                      "{s57}"(uni(11u + 3u * ps)), "{s58}"(uni(12u + 3u * ps)), "{s56}"(uni(ps ? VIMG - 0xc000u : KV0 - 0x1e000u)), "{v103}"(pl), "{v97}"(foffs), "{v[114:115]}"(fdat), "{s[46:47]}"(sm)
                    : MEM, "scc", "vcc", "v64","v65","v66","v67","v68","v69","v70","v71","v72","v73","v74","v75","v76","v77","v78","v79","v80","v81","v82","v83","v84","v85","v86","v87","v88","v89","v90","v91","v92","v93","v94","v95", "v102", "s59");
                __builtin_amdgcn_s_setprio(3);
                post(po + 32, ps + 1);
                auto fin = [&](const f32x16& a, int s2) {
                    u32x4 o;
#pragma unroll
                    for (int i = 0; i < 4; i++) o[i] = pk(mish(a[s2 * 8 + 2 * i]), mish(a[s2 * 8 + 2 * i + 1]));
                    return o;
                };
                if (ps) {
                    const unsigned adst = WS_ACT + ((rtile * 112 + ua * 4) * 64 + lane) * 16;
                    st16(wsr, adst, y0); st16(wsr, adst + 1024, y1); st16(wsr, adst + 2048, y2); st16(wsr, adst + 3072, y3);
                }
                y0 = fin(acc0, 0); y1 = fin(acc0, 1);
                if (!single) { y2 = fin(acc1, 0); y3 = fin(acc1, 1); }
            }
            const unsigned adst = WS_ACT + ((rtile * 112 + ub * 4) * 64 + lane) * 16;
            if (lane < 2) st4(wsr, WS_SM + SM_G + (tok + lane) * 4, f2u(gate));
            st16(wsr, adst + (j4 == 3 ? 2048 : 0), y0); st16(wsr, adst + (j4 == 3 ? 3072 : 1024), y1);
            if (j4 < 2) { st16(wsr, adst + 2048, y2); st16(wsr, adst + 3072, y3); }
        }
      }
        LANEVALS;
        __builtin_amdgcn_s_setprio(HPRIO);
        wait_ge_s<HPRIO>(pw + 64, 1);
        LDC(2) LDC(3) LDC(4) LDC(5) LDC(6) LDC(7)
        const int qt = qb * 8 + 2 * lw;
        st16(wsr, WS_O + (qt * 32 + head) * 1024 + lo16, (u32x4){~0u, ~0u, ~0u, ~0u});
        st16(wsr, WS_O + ((qt + 1) * 32 + head) * 1024 + lo16, (u32x4){~0u, ~0u, ~0u, ~0u});
        wait_ge_s<HPRIO>(pl + 32, 2);
        WAITV(22); STC(2, KV0 + 0x8000) post(po + 80, 3); WAITV(18); STC(3, KV0 + 0x8000) post(po + 80, 4);
        WAITV(14); STC(4, 0) WAITV(10); STC(5, 0) WAITV(6); STC(6, 0x8000) WAITV(2); STC(7, VIMG - 0x4000)
        WAITV(0);
        post(po + 80, 9);
        if (lane < 2) st4(wsr, WS_SM + (lane ? SM_FC + (unsigned)((qt >> 1) * 32 + head) * 128u : SM_FB + (unsigned)(rtile * 16 + c16) * 128u), tag);
        const unsigned poff = (unsigned)(lr * 1024 + cb * 128) + wsw16;
        auto piece = [&](int n) {
            const char* src = n < 7 ? (const char*)w2 + (lw * 7 + n) * 65536 : (const char*)wo + (lw * 2 + n - 7) * 65536;
#pragma unroll
            for (int k = 0; k < 8; k++) glds16(src + k * 8192, poff, sbase + slot_lds(lw, n == 8 ? 2 : (n & 1)) + k * 1024);
        };
        wait_ge_s<HPRIO>(pl + 96, (lw + 1) >> 2);
        piece(0);
        poll_tag(sm, h ? SM_FC + (unsigned)(rb * 32 + lw * 8 + (lane & 7)) * 128u : SM_FB + (unsigned)(rb * 32 + r) * 128u, uni(tag));
        float g0, g1;
        asm volatile("global_load_dword %0, %2, %3 sc1\n\tglobal_load_dword %1, %2, %3 offset:128 sc1" : "=&v"(g0), "=&v"(g1) : "v"(SM_G + (unsigned)(rb * 64 + r) * 4u), "s"(sm) : MEM);
        const char* a0p = ws + WS_ACT + (rb * 2 * 112 + lw * 28) * 1024;
        const char* a1p = a0p + 112 * 1024;
#define LDA(n) { const char* p0_ = a0p + n * 4096; const char* p1_ = a1p + n * 4096; constexpr int B_ = 8 * (n % 3); \
            lda4<B_, 0>(p0_, lo16); lda4<B_ + 1, 0>(p1_, lo16); lda4<B_ + 2, 1024>(p0_, lo16); lda4<B_ + 3, 1024>(p1_, lo16); \
            lda4<B_ + 4, 2048>(p0_, lo16); lda4<B_ + 5, 2048>(p1_, lo16); lda4<B_ + 6, 3072>(p0_, lo16); lda4<B_ + 7, 3072>(p1_, lo16); }
        LDA(0) LDA(1) LDA(2)
        wait_ge_s<HPRIO>(pl + 96, ((lw + 1) >> 1) + 1);
        piece(1);
        const unsigned s0 = wr0 + slot_lds(lw, 0), s1 = wr0 + slot_lds(lw, 1);
        f32x16 w0, w1;
        WAITV(8);  g2_read(w0, w1, s0); piece(2); g2_first<0, HPRIO>(b0, b1v, w0, w1); LDA(3)
        WAITV(16); g2_read(w0, w1, s1); piece(3); g2_mma<32, HPRIO>(b0, b1v, w0, w1);  LDA(4)
        WAITV(24); g2_read(w0, w1, s0); piece(4); g2_mma<64, HPRIO>(b0, b1v, w0, w1);  LDA(5)
        WAITV(24); g2_read(w0, w1, s1); piece(5); g2_mma<0, HPRIO>(b0, b1v, w0, w1);   LDA(6)
        WAITV(24); g2_read(w0, w1, s0); piece(6);
        f32x16 w2a, w3a;
        WAITV(16);
        asm volatile(
        "ds_read_b64_tr_b16 v[32:33], v96 offset:0\n"
        "ds_read_b64_tr_b16 v[34:35], v96 offset:1024\n"
        "ds_read_b64_tr_b16 v[36:37], v97 offset:0\n"
        "ds_read_b64_tr_b16 v[38:39], v97 offset:1024\n"
        "ds_read_b64_tr_b16 v[40:41], v96 offset:2048\n"
        "ds_read_b64_tr_b16 v[42:43], v96 offset:3072\n"
        "ds_read_b64_tr_b16 v[44:45], v97 offset:2048\n"
        "ds_read_b64_tr_b16 v[46:47], v97 offset:3072\n"
        "ds_read_b64_tr_b16 v[48:49], v96 offset:4096\n"
        "ds_read_b64_tr_b16 v[50:51], v96 offset:5120\n"
        "ds_read_b64_tr_b16 v[52:53], v97 offset:4096\n"
        "ds_read_b64_tr_b16 v[54:55], v97 offset:5120\n"
        "ds_read_b64_tr_b16 v[56:57], v96 offset:6144\n"
        "ds_read_b64_tr_b16 v[58:59], v96 offset:7168\n"
        "s_waitcnt lgkmcnt(12)\n"
        "ds_read_b64_tr_b16 v[60:61], v97 offset:6144\n"
        "ds_read_b64_tr_b16 v[62:63], v97 offset:7168\n"
        "s_waitcnt lgkmcnt(0)\n"
            : "={v[32:47]}"(w2a), "={v[48:63]}"(w3a) : "{v96}"(s1), "{v97}"(s1 ^ 64u) : MEM);
        piece(7);
        wait_ge_s<0>(pl + 96, 7);
        piece(8);
        asm volatile("" : "+v"(g0), "+v"(g1));
        *(LAS float*)(uintptr_t)(pw - 512 + r * 4) = g1;
        WAITV(0);
        wait_ge_s<1, 1>(po + 96, 8);
        asm volatile(
        "v_mfma_f32_32x32x16_bf16 v[0:15], v[64:67], a[32:35], v[0:15]\n"
        "v_mfma_f32_32x32x16_bf16 a[96:111], v[64:67], a[36:39], a[96:111]\n"
        "ds_read_b64_tr_b16 v[64:65], v96 offset:0\n"
        "ds_read_b64_tr_b16 v[66:67], v96 offset:1024\n"
        "v_mfma_f32_32x32x16_bf16 v[16:31], v[68:71], a[32:35], v[16:31]\n"
        "v_mfma_f32_32x32x16_bf16 a[112:127], v[68:71], a[36:39], a[112:127]\n"
        "ds_read_b64_tr_b16 v[68:69], v97 offset:0\n"
        "ds_read_b64_tr_b16 v[70:71], v97 offset:1024\n"
        "v_mfma_f32_32x32x16_bf16 v[0:15], v[72:75], a[40:43], v[0:15]\n"
        "v_mfma_f32_32x32x16_bf16 a[96:111], v[72:75], a[44:47], a[96:111]\n"
        "ds_read_b64_tr_b16 v[72:73], v96 offset:2048\n"
        "ds_read_b64_tr_b16 v[74:75], v96 offset:3072\n"
        "v_mfma_f32_32x32x16_bf16 v[16:31], v[76:79], a[40:43], v[16:31]\n"
        "v_mfma_f32_32x32x16_bf16 a[112:127], v[76:79], a[44:47], a[112:127]\n"
        "ds_read_b64_tr_b16 v[76:77], v97 offset:2048\n"
        "ds_read_b64_tr_b16 v[78:79], v97 offset:3072\n"
        "v_mfma_f32_32x32x16_bf16 v[0:15], v[80:83], a[48:51], v[0:15]\n"
        "v_mfma_f32_32x32x16_bf16 a[96:111], v[80:83], a[52:55], a[96:111]\n"
        "ds_read_b64_tr_b16 v[80:81], v96 offset:4096\n"
        "ds_read_b64_tr_b16 v[82:83], v96 offset:5120\n"
        "v_mfma_f32_32x32x16_bf16 v[16:31], v[84:87], a[48:51], v[16:31]\n"
        "v_mfma_f32_32x32x16_bf16 a[112:127], v[84:87], a[52:55], a[112:127]\n"
        "ds_read_b64_tr_b16 v[84:85], v97 offset:4096\n"
        "ds_read_b64_tr_b16 v[86:87], v97 offset:5120\n"
        "v_mfma_f32_32x32x16_bf16 v[0:15], v[88:91], a[56:59], v[0:15]\n"
        "v_mfma_f32_32x32x16_bf16 a[96:111], v[88:91], a[60:63], a[96:111]\n"
        "ds_read_b64_tr_b16 v[88:89], v96 offset:6144\n"
        "ds_read_b64_tr_b16 v[90:91], v96 offset:7168\n"
        "v_mfma_f32_32x32x16_bf16 v[16:31], v[92:95], a[56:59], v[16:31]\n"
        "v_mfma_f32_32x32x16_bf16 a[112:127], v[92:95], a[60:63], a[112:127]\n"
        "s_waitcnt lgkmcnt(12)\n"
        "ds_read_b64_tr_b16 v[92:93], v97 offset:6144\n"
        "ds_read_b64_tr_b16 v[94:95], v97 offset:7168\n"
        "v_mfma_f32_32x32x16_bf16 v[0:15], v[32:35], a[64:67], v[0:15]\n"
        "v_mfma_f32_32x32x16_bf16 a[96:111], v[32:35], a[68:71], a[96:111]\n"
        "v_mfma_f32_32x32x16_bf16 v[16:31], v[36:39], a[64:67], v[16:31]\n"
        "v_mfma_f32_32x32x16_bf16 a[112:127], v[36:39], a[68:71], a[112:127]\n"
        "v_mfma_f32_32x32x16_bf16 v[0:15], v[40:43], a[72:75], v[0:15]\n"
        "v_mfma_f32_32x32x16_bf16 a[96:111], v[40:43], a[76:79], a[96:111]\n"
        "v_mfma_f32_32x32x16_bf16 v[16:31], v[44:47], a[72:75], v[16:31]\n"
        "v_mfma_f32_32x32x16_bf16 a[112:127], v[44:47], a[76:79], a[112:127]\n"
        "v_mfma_f32_32x32x16_bf16 v[0:15], v[48:51], a[80:83], v[0:15]\n"
        "v_mfma_f32_32x32x16_bf16 a[96:111], v[48:51], a[84:87], a[96:111]\n"
        "v_mfma_f32_32x32x16_bf16 v[16:31], v[52:55], a[80:83], v[16:31]\n"
        "v_mfma_f32_32x32x16_bf16 a[112:127], v[52:55], a[84:87], a[112:127]\n"
        "v_mfma_f32_32x32x16_bf16 v[0:15], v[56:59], a[88:91], v[0:15]\n"
        "v_mfma_f32_32x32x16_bf16 a[96:111], v[56:59], a[92:95], a[96:111]\n"
        "v_mfma_f32_32x32x16_bf16 v[16:31], v[60:63], a[88:91], v[16:31]\n"
        "v_mfma_f32_32x32x16_bf16 a[112:127], v[60:63], a[92:95], a[112:127]\n"
        "s_waitcnt lgkmcnt(0)\n"
        "v_mov_b32 v100, 1\n"
        "s_mov_b64 exec, 1\n"
        "ds_write_b32 v99, v100\n"
        "s_mov_b64 exec, -1\n"
        "v_mfma_f32_32x32x16_bf16 a[96:111], v[64:67], a[4:7], a[96:111]\n"
        "v_mfma_f32_32x32x16_bf16 a[112:127], v[68:71], a[4:7], a[112:127]\n"
        "v_mfma_f32_32x32x16_bf16 a[96:111], v[72:75], a[12:15], a[96:111]\n"
        "v_mfma_f32_32x32x16_bf16 a[112:127], v[76:79], a[12:15], a[112:127]\n"
        "v_mfma_f32_32x32x16_bf16 a[96:111], v[80:83], a[20:23], a[96:111]\n"
        "v_mfma_f32_32x32x16_bf16 a[112:127], v[84:87], a[20:23], a[112:127]\n"
        "v_mfma_f32_32x32x16_bf16 a[96:111], v[88:91], a[28:31], a[96:111]\n"
        "v_mfma_f32_32x32x16_bf16 a[112:127], v[92:95], a[28:31], a[112:127]\n"
        "v_mfma_f32_32x32x16_bf16 v[0:15], v[64:67], a[0:3], v[0:15]\n"
        "v_mfma_f32_32x32x16_bf16 v[16:31], v[68:71], a[0:3], v[16:31]\n"
        "s_nop 7\n"
        "s_nop 1\n"
        "v_mfma_f32_32x32x16_bf16 v[0:15], v[72:75], a[8:11], v[0:15]\n"
        "ds_write_b128 v98, a[96:99] offset:0\n"
        "ds_write_b128 v98, a[100:103] offset:1024\n"
        "v_mfma_f32_32x32x16_bf16 v[16:31], v[76:79], a[8:11], v[16:31]\n"
        "ds_write_b128 v98, a[104:107] offset:2048\n"
        "ds_write_b128 v98, a[108:111] offset:3072\n"
        "v_mfma_f32_32x32x16_bf16 v[0:15], v[80:83], a[16:19], v[0:15]\n"
        "ds_write_b128 v98, a[112:115] offset:4096\n"
        "ds_write_b128 v98, a[116:119] offset:5120\n"
        "v_mfma_f32_32x32x16_bf16 v[16:31], v[84:87], a[16:19], v[16:31]\n"
        "ds_write_b128 v98, a[120:123] offset:6144\n"
        "ds_write_b128 v98, a[124:127] offset:7168\n"
        "v_mfma_f32_32x32x16_bf16 v[0:15], v[88:91], a[24:27], v[0:15]\n"
        "v_mfma_f32_32x32x16_bf16 v[16:31], v[92:95], a[24:27], v[16:31]\n"
        "s_nop 7\n"
        "s_nop 4\n"
            : "+{v[0:15]}"(b0), "+{v[16:31]}"(b1v), "+{v[64:79]}"(w0), "+{v[80:95]}"(w1)
            : "{v[32:47]}"(w2a), "{v[48:63]}"(w3a), "{v96}"(s0), "{v97}"(s0 ^ 64u), "{v98}"(sbase + img_lds(1, lw) + lo16), "{v99}"(pw + 112 + lw * 4) : MEM, "v100");
        post(pw + 112 + lw * 4, 2);
        gv = g0;
        const unsigned lo16_ = lo16;
        O_LOADS;
    }

    int lane_; LANE_(lane_);
    const int r_ = lane_ & 31, h_ = lane_ >> 5, rt = w >> 2;
    const unsigned lo16_ = lane_ * 16;
#pragma unroll
    for (int i = 0; i < 16; i++) { b0[i] *= 1.0f - gv; b1v[i] *= 1.0f - gv; }
    asm volatile("" : "+v"(b0), "+v"(b1v));
    {
        const int q = (lane_ & 15) >> 2;
        const unsigned wq = sbase + (unsigned)((4 * h_ + q) * 128 + ((lane_ >> 4) & 1) * 32 + (lane_ & 3) * 8) + (unsigned)(((q >> 1) & 1) * 64);
        asm volatile(
        "s_waitcnt lgkmcnt(0)\n"
        "v_add_u32 v96, s65, v98\n"
        "v_add_u32 v97, s65, v99\n"
        "ds_read_b64_tr_b16 v[64:65], v96 offset:0\n"
        "ds_read_b64_tr_b16 v[66:67], v96 offset:1024\n"
        "ds_read_b64_tr_b16 v[68:69], v97 offset:0\n"
        "ds_read_b64_tr_b16 v[70:71], v97 offset:1024\n"
        "ds_read_b64_tr_b16 v[72:73], v96 offset:2048\n"
        "ds_read_b64_tr_b16 v[74:75], v96 offset:3072\n"
        "ds_read_b64_tr_b16 v[76:77], v97 offset:2048\n"
        "ds_read_b64_tr_b16 v[78:79], v97 offset:3072\n"
        "ds_read_b64_tr_b16 v[80:81], v96 offset:4096\n"
        "ds_read_b64_tr_b16 v[82:83], v96 offset:5120\n"
        "ds_read_b64_tr_b16 v[84:85], v97 offset:4096\n"
        "ds_read_b64_tr_b16 v[86:87], v97 offset:5120\n"
        "ds_read_b64_tr_b16 v[88:89], v96 offset:6144\n"
        "ds_read_b64_tr_b16 v[90:91], v96 offset:7168\n"
        "s_mov_b32 s58, 0x10000\n"
        "LT5_%=:\n"
        "s_waitcnt vmcnt(0)\n"
        "v_max3_u32 v104, v32, v33, v34\n"
        "v_max_u32 v104, v104, v35\n"
        "v_max3_u32 v105, v36, v37, v38\n"
        "v_max_u32 v105, v105, v39\n"
        "v_max3_u32 v106, v40, v41, v42\n"
        "v_max_u32 v106, v106, v43\n"
        "v_max3_u32 v107, v44, v45, v46\n"
        "v_max_u32 v107, v107, v47\n"
        "v_max3_u32 v108, v48, v49, v50\n"
        "v_max_u32 v108, v108, v51\n"
        "v_max3_u32 v109, v52, v53, v54\n"
        "v_max_u32 v109, v109, v55\n"
        "v_max3_u32 v110, v56, v57, v58\n"
        "v_max_u32 v110, v110, v59\n"
        "v_max3_u32 v111, v60, v61, v62\n"
        "v_max_u32 v111, v111, v63\n"
        "v_max3_u32 v103, v104, v105, v106\n"
        "v_max3_u32 v103, v103, v107, v108\n"
        "v_max3_u32 v103, v103, v109, v110\n"
        "v_max_u32 v103, v103, v111\n"
        "v_cmp_eq_u32 vcc, -1, v103\n"
        "s_nop 1\n"
        "s_cmp_eq_u64 vcc, 0\n"
        "s_cbranch_scc1 LT6_%=\n"
        "s_sub_u32 s58, s58, 1\n"
        "s_cbranch_scc1 LT6_%=\n"
        "s_sleep 2\n"
        "v_cmp_eq_u32 vcc, -1, v104\n"
        "s_nop 1\n"
        "s_cmp_eq_u64 vcc, 0\n"
        "s_cbranch_scc1 LT70_%=\n"
        "global_load_dwordx4 v[32:35], v100, s[40:41] offset:0 sc1\n"
        "LT70_%=:\n"
        "v_cmp_eq_u32 vcc, -1, v105\n"
        "s_nop 1\n"
        "s_cmp_eq_u64 vcc, 0\n"
        "s_cbranch_scc1 LT71_%=\n"
        "global_load_dwordx4 v[36:39], v100, s[40:41] offset:1024 sc1\n"
        "LT71_%=:\n"
        "v_cmp_eq_u32 vcc, -1, v106\n"
        "s_nop 1\n"
        "s_cmp_eq_u64 vcc, 0\n"
        "s_cbranch_scc1 LT72_%=\n"
        "global_load_dwordx4 v[40:43], v100, s[40:41] offset:2048 sc1\n"
        "LT72_%=:\n"
        "v_cmp_eq_u32 vcc, -1, v107\n"
        "s_nop 1\n"
        "s_cmp_eq_u64 vcc, 0\n"
        "s_cbranch_scc1 LT73_%=\n"
        "global_load_dwordx4 v[44:47], v100, s[40:41] offset:3072 sc1\n"
        "LT73_%=:\n"
        "v_cmp_eq_u32 vcc, -1, v108\n"
        "s_nop 1\n"
        "s_cmp_eq_u64 vcc, 0\n"
        "s_cbranch_scc1 LT74_%=\n"
        "global_load_dwordx4 v[48:51], v101, s[40:41] offset:0 sc1\n"
        "LT74_%=:\n"
        "v_cmp_eq_u32 vcc, -1, v109\n"
        "s_nop 1\n"
        "s_cmp_eq_u64 vcc, 0\n"
        "s_cbranch_scc1 LT75_%=\n"
        "global_load_dwordx4 v[52:55], v101, s[40:41] offset:1024 sc1\n"
        "LT75_%=:\n"
        "v_cmp_eq_u32 vcc, -1, v110\n"
        "s_nop 1\n"
        "s_cmp_eq_u64 vcc, 0\n"
        "s_cbranch_scc1 LT76_%=\n"
        "global_load_dwordx4 v[56:59], v101, s[40:41] offset:2048 sc1\n"
        "LT76_%=:\n"
        "v_cmp_eq_u32 vcc, -1, v111\n"
        "s_nop 1\n"
        "s_cmp_eq_u64 vcc, 0\n"
        "s_cbranch_scc1 LT77_%=\n"
        "global_load_dwordx4 v[60:63], v101, s[40:41] offset:3072 sc1\n"
        "LT77_%=:\n"
        "s_branch LT5_%=\n"
        "LT6_%=:\n"
        "s_waitcnt lgkmcnt(12)\n"
        "v_mfma_f32_32x32x16_bf16 a[0:15], v[64:67], v[32:35], 0\n"
        "ds_read_b64_tr_b16 v[92:93], v97 offset:6144\n"
        "ds_read_b64_tr_b16 v[94:95], v97 offset:7168\n"
        "s_waitcnt lgkmcnt(12)\n"
        "v_mfma_f32_32x32x16_bf16 a[16:31], v[68:71], v[32:35], 0\n"
        "v_add_u32 v96, s66, v98\n"
        "v_add_u32 v97, s66, v99\n"
        "ds_read_b64_tr_b16 v[64:65], v96 offset:0\n"
        "ds_read_b64_tr_b16 v[66:67], v96 offset:1024\n"
        "s_waitcnt lgkmcnt(12)\n"
        "v_mfma_f32_32x32x16_bf16 a[0:15], v[72:75], v[36:39], a[0:15]\n"
        "ds_read_b64_tr_b16 v[68:69], v97 offset:0\n"
        "ds_read_b64_tr_b16 v[70:71], v97 offset:1024\n"
        "s_waitcnt lgkmcnt(12)\n"
        "v_mfma_f32_32x32x16_bf16 a[16:31], v[76:79], v[36:39], a[16:31]\n"
        "ds_read_b64_tr_b16 v[72:73], v96 offset:2048\n"
        "ds_read_b64_tr_b16 v[74:75], v96 offset:3072\n"
        "s_waitcnt lgkmcnt(12)\n"
        "v_mfma_f32_32x32x16_bf16 a[0:15], v[80:83], v[40:43], a[0:15]\n"
        "ds_read_b64_tr_b16 v[76:77], v97 offset:2048\n"
        "ds_read_b64_tr_b16 v[78:79], v97 offset:3072\n"
        "s_waitcnt lgkmcnt(12)\n"
        "v_mfma_f32_32x32x16_bf16 a[16:31], v[84:87], v[40:43], a[16:31]\n"
        "ds_read_b64_tr_b16 v[80:81], v96 offset:4096\n"
        "ds_read_b64_tr_b16 v[82:83], v96 offset:5120\n"
        "s_waitcnt lgkmcnt(12)\n"
        "v_mfma_f32_32x32x16_bf16 a[0:15], v[88:91], v[44:47], a[0:15]\n"
        "ds_read_b64_tr_b16 v[84:85], v97 offset:4096\n"
        "ds_read_b64_tr_b16 v[86:87], v97 offset:5120\n"
        "s_waitcnt lgkmcnt(12)\n"
        "v_mfma_f32_32x32x16_bf16 a[16:31], v[92:95], v[44:47], a[16:31]\n"
        "ds_read_b64_tr_b16 v[88:89], v96 offset:6144\n"
        "ds_read_b64_tr_b16 v[90:91], v96 offset:7168\n"
        "s_waitcnt lgkmcnt(12)\n"
        "v_mfma_f32_32x32x16_bf16 a[0:15], v[64:67], v[48:51], a[0:15]\n"
        "ds_read_b64_tr_b16 v[92:93], v97 offset:6144\n"
        "ds_read_b64_tr_b16 v[94:95], v97 offset:7168\n"
        "s_waitcnt lgkmcnt(12)\n"
        "v_mfma_f32_32x32x16_bf16 a[16:31], v[68:71], v[48:51], a[16:31]\n"
        "s_waitcnt lgkmcnt(10)\n"
        "v_mfma_f32_32x32x16_bf16 a[0:15], v[72:75], v[52:55], a[0:15]\n"
        "s_waitcnt lgkmcnt(8)\n"
        "v_mfma_f32_32x32x16_bf16 a[16:31], v[76:79], v[52:55], a[16:31]\n"
        "s_waitcnt lgkmcnt(6)\n"
        "v_mfma_f32_32x32x16_bf16 a[0:15], v[80:83], v[56:59], a[0:15]\n"
        "s_waitcnt lgkmcnt(4)\n"
        "v_mfma_f32_32x32x16_bf16 a[16:31], v[84:87], v[56:59], a[16:31]\n"
        "s_waitcnt lgkmcnt(2)\n"
        "v_mfma_f32_32x32x16_bf16 a[0:15], v[88:91], v[60:63], a[0:15]\n"
        "s_waitcnt lgkmcnt(0)\n"
        "v_mfma_f32_32x32x16_bf16 a[16:31], v[92:95], v[60:63], a[16:31]\n"
        "s_nop 7\n"
        "s_nop 3\n"
        "v_accvgpr_read_b32 v32, a0\n"
        "v_accvgpr_read_b32 v33, a1\n"
        "v_accvgpr_read_b32 v34, a2\n"
        "v_accvgpr_read_b32 v35, a3\n"
        "v_accvgpr_read_b32 v36, a4\n"
        "v_accvgpr_read_b32 v37, a5\n"
        "v_accvgpr_read_b32 v38, a6\n"
        "v_accvgpr_read_b32 v39, a7\n"
        "v_accvgpr_read_b32 v40, a8\n"
        "v_accvgpr_read_b32 v41, a9\n"
        "v_accvgpr_read_b32 v42, a10\n"
        "v_accvgpr_read_b32 v43, a11\n"
        "v_accvgpr_read_b32 v44, a12\n"
        "v_accvgpr_read_b32 v45, a13\n"
        "v_accvgpr_read_b32 v46, a14\n"
        "v_accvgpr_read_b32 v47, a15\n"
        "v_accvgpr_read_b32 v48, a16\n"
        "v_accvgpr_read_b32 v49, a17\n"
        "v_accvgpr_read_b32 v50, a18\n"
        "v_accvgpr_read_b32 v51, a19\n"
        "v_accvgpr_read_b32 v52, a20\n"
        "v_accvgpr_read_b32 v53, a21\n"
        "v_accvgpr_read_b32 v54, a22\n"
        "v_accvgpr_read_b32 v55, a23\n"
        "v_accvgpr_read_b32 v56, a24\n"
        "v_accvgpr_read_b32 v57, a25\n"
        "v_accvgpr_read_b32 v58, a26\n"
        "v_accvgpr_read_b32 v59, a27\n"
        "v_accvgpr_read_b32 v60, a28\n"
        "v_accvgpr_read_b32 v61, a29\n"
        "v_accvgpr_read_b32 v62, a30\n"
        "v_accvgpr_read_b32 v63, a31\n"
            : "+{v[32:35]}"(of0), "+{v[36:39]}"(of1), "+{v[40:43]}"(of2), "+{v[44:47]}"(of3), "+{v[48:51]}"(of4), "+{v[52:55]}"(of5), "+{v[56:59]}"(of6), "+{v[60:63]}"(of7)
            : "{v98}"(wq), "{v99}"(wq ^ 64u), "{v100}"(lo16_), "{v101}"(lo16_ + 4096u), "{s[40:41]}"(ob), "{s65}"(uni(slot_lds(lw, 1))), "{s66}"(uni(slot_lds(lw, 2)))
            : MEM, "scc", "vcc", "s58", "v64","v65","v66","v67","v68","v69","v70","v71","v72","v73","v74","v75","v76","v77","v78","v79","v80","v81","v82","v83","v84","v85","v86","v87","v88","v89","v90","v91","v92","v93","v94","v95","v96","v97", "v103","v104","v105","v106","v107","v108","v109","v110","v111");
    }
    {
        const unsigned rw = sbase + img_lds(rt, lw) + lo16_;
        auto put = [&](const u32x4& a, const f32x16& b, int i, int qd) {
            f32x4 u;
#pragma unroll
            for (int e = 0; e < 4; e++) u[e] = __builtin_fmaf(gv, u2f(a[e]), b[4 * i + e]);
            *(LAS f32x4*)(uintptr_t)((rw ^ (qd * 32u)) + qd * 1024) = u;
        };
        put(of0, b0, 0, 0); put(of1, b0, 1, 1); put(of2, b0, 2, 2); put(of3, b0, 3, 3);
        put(of4, b1v, 0, 4); put(of5, b1v, 1, 5); put(of6, b1v, 2, 6); put(of7, b1v, 3, 7);
    }
    barrier();
    if (bid == 0 && w == 0 && lane_ == 0) st4(wsr, WS_SM + SM_EP, tag);
    {
        const int tk = lw * 8 + (lane_ >> 3), qd = lane_ & 7;
        const unsigned ra = sbase + qd * 1024 + ((tk * 16) ^ (qd * 32));
        f32x4 lo4 = {0.f, 0.f, 0.f, 0.f}, hi4 = lo4;
#pragma unroll
        for (int k = 0; k < 4; k++) {
            lo4 += *(const LAS f32x4*)(uintptr_t)(ra + img_lds(rt, k));
            hi4 += *(const LAS f32x4*)(uintptr_t)(ra + img_lds(rt, k) + 512);
        }
        u32x4 ov = {pk(lo4[0], lo4[1]), pk(lo4[2], lo4[3]), pk(hi4[0], hi4[1]), pk(hi4[2], hi4[3])};
        __builtin_amdgcn_raw_buffer_store_b128(ov, mkrsrc(out, 2u << 20), (unsigned)((rb * 64 + rt * 32 + tk) * 1024 + cb * 128 + qd * 16), 0, 16);
    }
    asm volatile("Lcode_last:");
}

#if defined(__HIP_DEVICE_COMPILE__)
#pragma clang attribute pop
#endif

extern "C" void kernel_launch(void* const* d_in, const int* in_sizes, int n_in, void* d_out, int out_size, void* d_ws, size_t ws_size,
                              hipStream_t stream) {
    (void)hipFuncSetAttribute((const void*)fused, hipFuncAttributeMaxDynamicSharedMemorySize, LDS_BYTES);
    fused<<<256, 512, LDS_BYTES, stream>>>((const __bf16*)d_in[0], (const __bf16*)d_in[1], (const __bf16*)d_in[2], (const __bf16*)d_in[4],
                                           (const __bf16*)d_in[5], (const __bf16*)d_in[6], (const __bf16*)d_in[7], (char*)d_ws, (__bf16*)d_out);
}
```
